```python
import torch

def _softmax(x, axis=-1):
    return torch.softmax(x, dim=axis)
def _gelu(x):
    return torch.nn.functional.gelu(x, approximate="tanh")
def _silu(x):
    return torch.nn.functional.silu(x)
def _sigmoid(x):
    return torch.sigmoid(x)
def _layernorm(x, axis=-1, eps=1e-5):
    m = x.mean(dim=axis, keepdim=True)
    v = x.var(dim=axis, keepdim=True, unbiased=False)
    return (x - m) / torch.sqrt(v + eps)
def _scatter_add(tgt, idx, src, axis=0):
    return tgt.clone().index_add_(axis, idx, src)
def _segment_sum(src, seg, n):
    out = torch.zeros((n,) + src.shape[1:], dtype=src.dtype)
    return out.index_add_(0, seg, src)
def _pad(x, pw):
    flat = tuple(v for pair in reversed(pw) for v in pair)
    return torch.nn.functional.pad(x, flat)

kernel_name = 'concat_heads__gqa_block_cached__sliding_block_cached__decode'

def setup_inputs():

    _w = (lambda a: (lambda t: t.to(torch.bfloat16) if t.is_floating_point() else t)(torch.as_tensor(a)))
    def _A_setup():
        B, Sq, D = 4, 1, 1024
        Hq, Hkv, Sk, Dh = 32, 4, 8192, 128
        assert Hq % Hkv == 0, (Hq, Hkv)
        return {
            "x": _w((lambda *s: torch.randn(*s, dtype=torch.bfloat16) * 0.8)(B, Sq, D)),
            "k_cache": _w((lambda *s: torch.randn(*s, dtype=torch.bfloat16) * 1.6)(B, Hkv, Sk, Dh)),
            "v_cache": _w((lambda *s: torch.randn(*s, dtype=torch.bfloat16) * 0.2)(B, Hkv, Sk, Dh)),
            "wq": _w((lambda *s: torch.randn(*s, dtype=torch.bfloat16) * 1.6)(D, Hq * Dh) * (D ** -0.5)),
            "wo": _w((lambda *s: torch.randn(*s, dtype=torch.bfloat16) * 0.2)(Hq * Dh, D) * ((Hq * Dh) ** -0.5)),
        }
    _A_in = _A_setup()

    def _B_setup():
        B, Sq, D = 4, 1, 1024
        Hq, Hkv, Sk, Dh = 32, 4, 8192, 128
        assert Hq % Hkv == 0, (Hq, Hkv)
        return {
            "x": _w((lambda *s: torch.randn(*s, dtype=torch.bfloat16) * 0.8)(B, Sq, D)),
            "k_cache": _w((lambda *s: torch.randn(*s, dtype=torch.bfloat16) * 1.6)(B, Hkv, Sk, Dh)),
            "v_cache": _w((lambda *s: torch.randn(*s, dtype=torch.bfloat16) * 0.2)(B, Hkv, Sk, Dh)),
            "wq": _w((lambda *s: torch.randn(*s, dtype=torch.bfloat16) * 1.6)(D, Hq * Dh) * (D ** -0.5)),
            "wo": _w((lambda *s: torch.randn(*s, dtype=torch.bfloat16) * 0.2)(Hq * Dh, D) * ((Hq * Dh) ** -0.5)),
        }
    _B_in = _B_setup()

    return {
        "A_x": _A_in["x"],
        "A_k_cache": _A_in["k_cache"],
        "A_v_cache": _A_in["v_cache"],
        "A_wq": _A_in["wq"],
        "A_wo": _A_in["wo"],
        "B_x": _B_in["x"],
        "B_k_cache": _B_in["k_cache"],
        "B_v_cache": _B_in["v_cache"],
        "B_wq": _B_in["wq"],
        "B_wo": _B_in["wo"],
    }

def reference(A_x, A_k_cache, A_v_cache, A_wq, A_wo, B_x, B_k_cache, B_v_cache, B_wq, B_wo):
    def _A_ref(x, k_cache, v_cache, wq, wo):
        B, Sq, D = x.shape
        Hq, Dh = 32, 128
        Hkv = k_cache.shape[1]
        rep = Hq // Hkv
        q = (lambda x, *d: x.permute(*d))((x @ wq).reshape(B, Sq, Hq, Dh), 0, 2, 1, 3)
        k = (lambda x, n, axis: torch.repeat_interleave(x, n, dim=axis))(k_cache, rep, axis=1)
        v = (lambda x, n, axis: torch.repeat_interleave(x, n, dim=axis))(v_cache, rep, axis=1)
        scores = torch.einsum("bhqd,bhkd->bhqk", q, k) * (Dh ** -0.5)
        p = _softmax(scores, axis=-1)
        o = torch.einsum("bhqk,bhkd->bhqd", p, v)
        return (lambda x, *d: x.permute(*d))(o, 0, 2, 1, 3).reshape(B, Sq, Hq * Dh) @ wo

    def _B_ref(x, k_cache, v_cache, wq, wo):
        B, Sq, D = x.shape
        Hq, Dh = 32, 128
        Hkv, Sk = k_cache.shape[1], k_cache.shape[2]
        W = min(4096, max(256, Sk // 4))
        rep = Hq // Hkv
        q = (lambda x, *d: x.permute(*d))((x @ wq).reshape(B, Sq, Hq, Dh), 0, 2, 1, 3)
        k = (lambda x, n, axis: torch.repeat_interleave(x, n, dim=axis))(k_cache[:, :, Sk - W:, :], rep, axis=1)
        v = (lambda x, n, axis: torch.repeat_interleave(x, n, dim=axis))(v_cache[:, :, Sk - W:, :], rep, axis=1)
        scores = torch.einsum("bhqd,bhkd->bhqk", q, k) * (Dh ** -0.5)
        p = _softmax(scores, axis=-1)
        o = torch.einsum("bhqk,bhkd->bhqd", p, v)
        return (lambda x, *d: x.permute(*d))(o, 0, 2, 1, 3).reshape(B, Sq, Hq * Dh) @ wo

    a = _A_ref(A_x, A_k_cache, A_v_cache, A_wq, A_wo)
    b = _B_ref(B_x, B_k_cache, B_v_cache, B_wq, B_wo)
    return torch.concatenate([a, b], axis=-1)
```

```cpp
#include <hip/hip_runtime.h>
#include <hip/hip_bf16.h>
#include <stdint.h>

typedef unsigned v4u __attribute__((ext_vector_type(4)));
typedef unsigned v2u __attribute__((ext_vector_type(2)));
typedef float f4 __attribute__((ext_vector_type(4)));
typedef __bf16 bf8 __attribute__((ext_vector_type(8)));
typedef short s4 __attribute__((ext_vector_type(4)));
typedef __amdgpu_buffer_rsrc_t rsrc_t;
typedef unsigned long long u64;

#define STAMPS 0

constexpr int NWG = 256, NTHR = 512;
constexpr int SK = 8192, WIN = 2048, DH = 128, DM = 1024, NQ = 4096;
constexpr unsigned WS_EPOCH = 0;
constexpr unsigned WS_FLAG = 4096;
constexpr unsigned WS_QP = 8192;
constexpr unsigned WS_ML = WS_QP + 2 * 2 * 4 * 4096 * 4;
constexpr unsigned WS_OP = WS_ML + 2 * 4 * 32 * 16 * 8;
constexpr unsigned WS_P2 = WS_OP + 2 * 4 * 32 * 16 * 512;
constexpr unsigned WS_END = WS_P2 + 2 * 2 * 64 * 64 * 128;
constexpr unsigned WS_STAMP = WS_END;
constexpr int AUX_NT = 2, AUX_SC1 = 16;
constexpr unsigned SENT = 0xFFFFFFFFu;
constexpr unsigned SPIN_CAP = 1u << 16;

struct Params {
  const void *xA, *kA, *vA, *wqA, *woA, *xB, *kB, *vB, *wqB, *woB;
  void *out, *ws;
};

__device__ __forceinline__ rsrc_t mk(const void* p, unsigned bytes) { return __builtin_amdgcn_make_buffer_rsrc((void*)p, 0, (int)bytes, 0x00020000); }
__device__ __forceinline__ v4u ldnt(rsrc_t r, int voff, int soff) { return __builtin_amdgcn_raw_buffer_load_b128(r, voff, soff, AUX_NT); }
__device__ __forceinline__ v4u ldc(rsrc_t r, int voff, int soff) { return __builtin_amdgcn_raw_buffer_load_b128(r, voff, soff, AUX_SC1); }
__device__ __forceinline__ void stc(v4u v, rsrc_t r, int voff, int soff) { __builtin_amdgcn_raw_buffer_store_b128(v, r, voff, soff, AUX_SC1); }
__device__ __forceinline__ f4 mfma32(v4u a, v4u b, f4 c) { return __builtin_amdgcn_mfma_f32_16x16x32_bf16(__builtin_bit_cast(bf8, a), __builtin_bit_cast(bf8, b), c, 0, 0, 0); }
__device__ __forceinline__ f4 mfma16(v2u a, v2u b, f4 c) { return __builtin_amdgcn_mfma_f32_16x16x16bf16_1k(__builtin_bit_cast(s4, a), __builtin_bit_cast(s4, b), c, 0, 0, 0); }
__device__ __forceinline__ unsigned fu(float x) { return __builtin_bit_cast(unsigned, x); }
__device__ __forceinline__ float uf(unsigned x) { return __builtin_bit_cast(float, x); }
__device__ __forceinline__ unsigned ror8u(unsigned x) { return (unsigned)__builtin_amdgcn_mov_dpp((int)x, 0x128, 0xf, 0xf, true); }
__device__ __forceinline__ float ror8(float x) { return uf(ror8u(fu(x))); }
__device__ __forceinline__ unsigned pk2(float lo, float hi) {
  typedef __bf16 bf2 __attribute__((ext_vector_type(2)));
  typedef float f2 __attribute__((ext_vector_type(2)));
  f2 v = {lo, hi};
  return __builtin_bit_cast(unsigned, __builtin_convertvector(v, bf2));
}
template <int T> __device__ __forceinline__ unsigned tr(const v4u& a, const v4u& b) {
  return __builtin_amdgcn_perm(b[T >> 1], a[T >> 1], (T & 1) ? 0x07060302u : 0x05040100u);
}
template <int T> __device__ __forceinline__ v4u tr8(const v4u* r) { return v4u{tr<T>(r[0], r[1]), tr<T>(r[2], r[3]), tr<T>(r[4], r[5]), tr<T>(r[6], r[7])}; }
template <int T> __device__ __forceinline__ v2u tr4(const v4u* r) { return v2u{tr<T>(r[0], r[1]), tr<T>(r[2], r[3])}; }
__device__ __forceinline__ float red32(float a, float b) { auto s = __builtin_amdgcn_permlane32_swap(fu(a), fu(b), false, false); return uf(s[0]) + uf(s[1]); }
__device__ __forceinline__ float red16(float a, float b) { auto s = __builtin_amdgcn_permlane16_swap(fu(a), fu(b), false, false); return uf(s[0]) + uf(s[1]); }
__device__ __forceinline__ float max_x8_16_32(float x) {
  x = fmaxf(x, ror8(x));
  auto s = __builtin_amdgcn_permlane16_swap(fu(x), fu(x), false, false); x = fmaxf(uf(s[0]), uf(s[1]));
  auto t = __builtin_amdgcn_permlane32_swap(fu(x), fu(x), false, false); return fmaxf(uf(t[0]), uf(t[1]));
}
__device__ __forceinline__ float sum_x8_16_32(float x) { x = x + ror8(x); x = red16(x, x); return red32(x, x); }
__device__ __forceinline__ v4u san(f4 v) { v4u u = __builtin_bit_cast(v4u, v); return v4u{min(u[0], SENT - 1), min(u[1], SENT - 1), min(u[2], SENT - 1), min(u[3], SENT - 1)}; }
__device__ __forceinline__ unsigned mx4(const v4u& v) { return max(max(v[0], v[1]), max(v[2], v[3])); }
__device__ __forceinline__ bool wave_bad(bool bad) { return __builtin_amdgcn_ballot_w64(bad) != 0ull; }
#define PIN() __builtin_amdgcn_sched_barrier(0)
#define VMW(n) asm volatile("s_waitcnt vmcnt(" #n ")" ::: "memory")
#define BAR() do { asm volatile("s_waitcnt lgkmcnt(0)" ::: "memory"); __builtin_amdgcn_s_barrier(); asm volatile("" ::: "memory"); } while (0)

__device__ __forceinline__ void tr64(const v4u* wr, v4u* wt) {
  wt[0] = tr8<0>(wr); wt[1] = tr8<1>(wr); wt[2] = tr8<2>(wr); wt[3] = tr8<3>(wr);
  wt[4] = tr8<4>(wr); wt[5] = tr8<5>(wr); wt[6] = tr8<6>(wr); wt[7] = tr8<7>(wr);
}
__device__ __forceinline__ f4 tile64(const v4u* wt, v4u a, bool hi8) {
  const f4 z = {0.f, 0.f, 0.f, 0.f};
  f4 d0 = mfma32(a, wt[0], z), d4 = mfma32(a, wt[4], z), d1 = mfma32(a, wt[1], z), d5 = mfma32(a, wt[5], z);
  f4 d2 = mfma32(a, wt[2], z), d6 = mfma32(a, wt[6], z), d3 = mfma32(a, wt[3], z), d7 = mfma32(a, wt[7], z);
  f4 r;
  r[0] = (hi8 ? d4[1] : d0[0]) + ror8(hi8 ? d0[1] : d4[0]);
  r[1] = (hi8 ? d5[1] : d1[0]) + ror8(hi8 ? d1[1] : d5[0]);
  r[2] = (hi8 ? d6[1] : d2[0]) + ror8(hi8 ? d2[1] : d6[0]);
  r[3] = (hi8 ? d7[1] : d3[0]) + ror8(hi8 ? d3[1] : d7[0]);
  return r;
}
__device__ __forceinline__ void qk16(const v4u* kr, v4u q0, v4u q1, bool t0lane, float& z0, float& z1) {
  const f4 z = {0.f, 0.f, 0.f, 0.f};
  f4 da = mfma32(kr[1], q1, mfma32(kr[0], q0, z));
  f4 db = mfma32(kr[3], q1, mfma32(kr[2], q0, z));
  const float x0 = t0lane ? da[0] : db[0], x1 = t0lane ? da[1] : db[1], x2 = t0lane ? da[2] : db[2], x3 = t0lane ? da[3] : db[3];
  auto s = __builtin_amdgcn_permlane32_swap(fu(x0), fu(x1), false, false);
  auto t = __builtin_amdgcn_permlane32_swap(fu(x2), fu(x3), false, false);
  z0 = uf(s[0]) + ror8(uf(s[1]));
  z1 = uf(t[0]) + ror8(uf(t[1]));
}

constexpr int RSTR = 132;
constexpr int L_RED = 0;
constexpr int L_QR = L_RED + 2 * 8 * 8 * RSTR * 4;
constexpr int L_Q = L_QR + 8 * 1024;
constexpr int QSTR = 272;
constexpr int L_ML = L_Q + 2 * 8 * QSTR;
constexpr int L_O = L_ML + 2 * 8 * 8 * 8;
constexpr int L_END = L_O + 512;

#define STAMP(i)

__global__ void __launch_bounds__(NTHR, 2) fused_decode(Params p) {
  __shared__ __attribute__((aligned(16))) unsigned char lds[L_END];
  const int tid = threadIdx.x, lane = tid & 63, w = __builtin_amdgcn_readfirstlane(tid >> 6);
  const int j = blockIdx.x;
  const int G = lane >> 4, c16 = lane & 15;
  const bool hi8 = (lane & 8) != 0;
  const int br = j >> 7, i7 = j & 127;
  const int pair = j >> 4, b = pair >> 2, hkv = pair & 3, s = j & 15;
  const rsrc_t rws = mk(p.ws, WS_END);

  const int kh = i7 & 1, cs = i7 >> 1;
  v4u wr[8], wt[8];
  v4u xa;
  {
    const rsrc_t rx = mk(br ? p.xB : p.xA, 4 * DM * 2);
    const rsrc_t rq = mk(br ? p.wqB : p.wqA, DM * NQ * 2);
    const int r0 = 512 * kh + 64 * w;
    xa = __builtin_amdgcn_raw_buffer_load_b128(rx, ((c16 >> 2) * DM + 8 * (2 * G + (c16 & 1))) * 2, r0 * 2, 0);
    const int vo = (8 * (lane >> 3)) * (NQ * 2) + (lane & 7) * 16, so = r0 * (NQ * 2) + cs * 128;
#pragma unroll
    for (int e = 0; e < 8; ++e) wr[e] = ldnt(rq, vo, so + e * (NQ * 2));
  }
  PIN();
  const int ri = i7 >> 1, ch = i7 & 1;
  const int op_off = (((w >> 2) * 4 + b) * 32 + hkv * 8 + 2 * (w & 3) + (lane >> 5)) * (16 * 512) + s * 512 + (lane & 31) * 16;
  const int p2_off = (((br * 2 + ch) * 64 + 8 * w + (lane & 7)) * 64 + ri) * 128 + G * 32 + (hi8 ? 16 : 0);
  const int qp_off = (((br * 2 + kh) * 4 + G) * NQ + 64 * cs + 8 * (lane & 7) + (hi8 ? 4 : 0)) * 4;
  const int ml_off = ((((lane >> 3) & 1) * 4 + b) * 32 + hkv * 8 + (lane & 7)) * 128 + (s & 7) * 16 + (s >> 3) * 8;
  {
    const v4u sv = {SENT, SENT, SENT, SENT};
    stc(sv, rws, op_off, WS_OP);
    stc(sv, rws, p2_off, WS_P2);
    if (w == 0) stc(sv, rws, qp_off, WS_QP);
    if (w == 1 && lane < 16) __builtin_amdgcn_raw_buffer_store_b64(v2u{SENT, SENT}, rws, ml_off, WS_ML, AUX_SC1);
  }
  const v2u ep = __builtin_amdgcn_raw_buffer_load_b64(rws, 0, WS_EPOCH, AUX_SC1);
  PIN();
  const rsrc_t rkA = mk(p.kA, 16u * SK * DH * 2), rvA = mk(p.vA, 16u * SK * DH * 2);
  const rsrc_t rkB = mk(p.kB, 16u * SK * DH * 2), rvB = mk(p.vB, 16u * SK * DH * 2);
  const int soA = (pair * SK + s * 512 + w * 64) * 256, soB = (pair * SK + (SK - WIN) + s * 128 + w * 16) * 256;
  const int ko0 = (lane & 7) * 256 + (hi8 ? 64 : 0) + G * 16, ko1 = ko0 ^ 64;
  v4u kA[16], kB[4], vA[16], vB[4];
#define LDK(dst, r, so, T) do { dst[2 * (T)] = ldnt(r, ((T) & 1) ? ko1 : ko0, so + (T) * 2048); dst[2 * (T) + 1] = ldnt(r, ((T) & 1) ? ko1 : ko0, so + (T) * 2048 + 128); } while (0)
  LDK(kA, rkA, soA, 0); LDK(kA, rkA, soA, 1); LDK(kA, rkA, soA, 2); LDK(kA, rkA, soA, 3);
  PIN();
  STAMP(1);
  {
    tr64(wr, wt);
    const f4 r = tile64(wt, xa, hi8);
    *(f4*)(lds + L_QR + w * 1024 + lane * 16) = r;
  }
  VMW(8);
  BAR();
  STAMP(2);
  const u64 tag = (((u64)ep[1] << 32) | ep[0]) + 0x9E3779B97F4A7C15ull;
  if (w == 0) {
    f4 r = *(const f4*)(lds + L_QR + lane * 16);
#pragma unroll
    for (int u = 1; u < 8; ++u) r += *(const f4*)(lds + L_QR + u * 1024 + lane * 16);
    stc(san(r), rws, qp_off, WS_QP);
    if (lane == 0) __builtin_amdgcn_raw_buffer_store_b64(v2u{(unsigned)tag, (unsigned)(tag >> 32)}, rws, j * 8, WS_FLAG, AUX_SC1);
  }
  PIN();
  LDK(kA, rkA, soA, 4); LDK(kA, rkA, soA, 5); LDK(kA, rkA, soA, 6); LDK(kA, rkA, soA, 7);
  LDK(kB, rkB, soB, 0); LDK(kB, rkB, soB, 1);
  PIN();
  const int fl_off = (128 * (w >> 2) + 32 * ((lane >> 3) & 3) + 8 * (w & 3) + (lane & 7)) * 8;
  v2u fl = __builtin_amdgcn_raw_buffer_load_b64(rws, fl_off, WS_FLAG, AUX_SC1);
  PIN();
  const int vo = (4 * (G & 1) + (G >> 1)) * 256 + c16 * 16;
#define LDV(dst, r, so, e) dst[e] = ldnt(r, vo, so + (8 * ((e) >> 1) + 2 * ((e) & 1)) * 256)
  LDV(vB, rvB, soB, 0); LDV(vB, rvB, soB, 1); LDV(vB, rvB, soB, 2); LDV(vB, rvB, soB, 3);
  PIN();
  if (__builtin_expect(wave_bad(((fl[0] ^ (unsigned)tag) | (fl[1] ^ (unsigned)(tag >> 32))) != 0u), 0)) {
    unsigned spins = 0;
    do {
      __builtin_amdgcn_s_sleep(2);
      asm volatile("" ::: "memory");
      fl = __builtin_amdgcn_raw_buffer_load_b64(rws, fl_off, WS_FLAG, AUX_SC1);
    } while (wave_bad(((fl[0] ^ (unsigned)tag) | (fl[1] ^ (unsigned)(tag >> 32))) != 0u) && ++spins < SPIN_CAP);
  }
  __builtin_amdgcn_fence(__ATOMIC_ACQUIRE, "wavefront");
  PIN();
  STAMP(3);
  const int qg_off = (((w >> 2) * 2 * 4 + b) * NQ + hkv * 1024 + (w & 3) * 256 + lane * 4) * 4;
  v4u q0 = ldc(rws, qg_off, WS_QP), q1 = ldc(rws, qg_off, WS_QP + 4 * NQ * 4);
  PIN();
#pragma unroll
  for (int e = 0; e < 8; ++e) LDV(vA, rvA, soA, e);
  PIN();
  if (__builtin_expect(wave_bad(max(mx4(q0), mx4(q1)) == SENT), 0)) {
    unsigned spins = 0;
    do {
      __builtin_amdgcn_s_sleep(2);
      asm volatile("" ::: "memory");
      q0 = ldc(rws, qg_off, WS_QP); q1 = ldc(rws, qg_off, WS_QP + 4 * NQ * 4);
    } while (wave_bad(max(mx4(q0), mx4(q1)) == SENT) && ++spins < SPIN_CAP);
  }
  {
    const f4 qs = __builtin_bit_cast(f4, q0) + __builtin_bit_cast(f4, q1);
    *(v2u*)(lds + L_Q + ((w >> 2) * 8 + 2 * (w & 3) + (lane >> 5)) * QSTR + (lane & 31) * 8) = v2u{pk2(qs[0], qs[1]), pk2(qs[2], qs[3])};
  }
  BAR();
  STAMP(4);
  constexpr float C2 = 0.088388347648318440f * 1.4426950408889634f;
  unsigned pA[4], pB;
  {
    const unsigned char* qp = lds + L_Q + (lane & 7) * QSTR + (hi8 ? 64 : 0) + G * 16;
    const v4u qa0 = *(const v4u*)qp, qa1 = *(const v4u*)(qp + 128), qb0 = *(const v4u*)(qp + 8 * QSTR), qb1 = *(const v4u*)(qp + 8 * QSTR + 128);
    const bool t0lane = (G >> 1) == (c16 >> 3);
    float za[8], zb[2];
#pragma unroll
    for (int u = 0; u < 4; ++u) qk16(kA + 4 * u, qa0, qa1, t0lane, za[2 * u], za[2 * u + 1]);
    qk16(kB, qb0, qb1, t0lane, zb[0], zb[1]);
    float mA = fmaxf(fmaxf(fmaxf(za[0], za[1]), fmaxf(za[2], za[3])), fmaxf(fmaxf(za[4], za[5]), fmaxf(za[6], za[7])));
    float mB = fmaxf(zb[0], zb[1]);
    mA = max_x8_16_32(mA) * C2; mB = max_x8_16_32(mB) * C2;
    float lA = 0.f;
#pragma unroll
    for (int u = 0; u < 8; ++u) { za[u] = __builtin_amdgcn_exp2f(fmaf(za[u], C2, -mA)); lA += za[u]; }
    zb[0] = __builtin_amdgcn_exp2f(fmaf(zb[0], C2, -mB)); zb[1] = __builtin_amdgcn_exp2f(fmaf(zb[1], C2, -mB));
    float lB = zb[0] + zb[1];
    lA = sum_x8_16_32(lA); lB = sum_x8_16_32(lB);
    if (lane < 16) *(float2*)(lds + L_ML + (lane * 8 + w) * 8) = hi8 ? make_float2(mB, lB) : make_float2(mA, lA);
    BAR();
    float MA = -INFINITY, MB = -INFINITY;
    f4 ma[4], mb[4];
#pragma unroll
    for (int u = 0; u < 4; ++u) { ma[u] = *(const f4*)(lds + L_ML + (lane & 7) * 64 + u * 16); mb[u] = *(const f4*)(lds + L_ML + (8 + (lane & 7)) * 64 + u * 16); }
#pragma unroll
    for (int u = 0; u < 4; ++u) { MA = fmaxf(MA, fmaxf(ma[u][0], ma[u][2])); MB = fmaxf(MB, fmaxf(mb[u][0], mb[u][2])); }
    const float fA = __builtin_amdgcn_exp2f(mA - MA), fB = __builtin_amdgcn_exp2f(mB - MB);
#pragma unroll
    for (int u = 0; u < 4; ++u) pA[u] = pk2(za[2 * u] * fA, za[2 * u + 1] * fA);
    pB = pk2(zb[0] * fB, zb[1] * fB);
    if (w == 1) {
      const float M = hi8 ? MB : MA;
      float L = 0.f;
#pragma unroll
      for (int u = 0; u < 4; ++u) { const f4 v = hi8 ? mb[u] : ma[u]; L = fmaf(__builtin_amdgcn_exp2f(v[0] - M), v[1], L); L = fmaf(__builtin_amdgcn_exp2f(v[2] - M), v[3], L); }
      if (lane < 16) __builtin_amdgcn_raw_buffer_store_b64(v2u{min(fu(M), SENT - 1), min(fu(L), SENT - 1)}, rws, ml_off, WS_ML, AUX_SC1);
    }
  }
  PIN();
  STAMP(5);
#pragma unroll
  for (int e = 0; e < 8; ++e) vA[8 + e] = ldnt(rvA, vo, soA + 32 * 256 + (8 * (e >> 1) + 2 * (e & 1)) * 256);
  PIN();
  const int mg_off = ((br * 4 + (w >> 1)) * 32 + (ri >> 1)) * 128 + (lane >> 3) * 16;
  v4u ml = ldc(rws, mg_off, WS_ML);
  PIN();
  {
    const rsrc_t ro = mk(br ? p.woB : p.woA, NQ * DM * 2);
    const int vo2 = (8 * (lane >> 3)) * (DM * 2) + (lane & 7) * 16, so = (64 * ri) * (DM * 2) + (512 * ch + 64 * w) * 2;
#pragma unroll
    for (int e = 0; e < 8; ++e) wr[e] = ldnt(ro, vo2, so + e * (DM * 2));
  }
  PIN();
  {
    const f4 z = {0.f, 0.f, 0.f, 0.f};
    float* red = (float*)(lds + L_RED) + (w * 8 + (lane & 7)) * RSTR + 32 * G;
    {
      const v2u pb = {pB, ror8u(pB)};
      f4 o[8];
      o[0] = mfma16(tr4<0>(vB), pb, z); o[1] = mfma16(tr4<1>(vB), pb, z); o[2] = mfma16(tr4<2>(vB), pb, z); o[3] = mfma16(tr4<3>(vB), pb, z);
      o[4] = mfma16(tr4<4>(vB), pb, z); o[5] = mfma16(tr4<5>(vB), pb, z); o[6] = mfma16(tr4<6>(vB), pb, z); o[7] = mfma16(tr4<7>(vB), pb, z);
      if (!hi8) {
#pragma unroll
        for (int r = 0; r < 4; ++r) {
          *(f4*)(red + 64 * RSTR + 8 * r) = f4{o[0][r], o[1][r], o[2][r], o[3][r]};
          *(f4*)(red + 64 * RSTR + 8 * r + 4) = f4{o[4][r], o[5][r], o[6][r], o[7][r]};
        }
      }
    }
    PIN();
    {
      const v4u pa0 = {pA[0], ror8u(pA[0]), pA[1], ror8u(pA[1])}, pa1 = {pA[2], ror8u(pA[2]), pA[3], ror8u(pA[3])};
      f4 o[8];
      o[0] = mfma32(tr8<0>(vA), pa0, z); o[1] = mfma32(tr8<1>(vA), pa0, z); o[2] = mfma32(tr8<2>(vA), pa0, z); o[3] = mfma32(tr8<3>(vA), pa0, z);
      o[4] = mfma32(tr8<4>(vA), pa0, z); o[5] = mfma32(tr8<5>(vA), pa0, z); o[6] = mfma32(tr8<6>(vA), pa0, z); o[7] = mfma32(tr8<7>(vA), pa0, z);
      PIN();
      STAMP(6);
      o[0] = mfma32(tr8<0>(vA + 8), pa1, o[0]); o[1] = mfma32(tr8<1>(vA + 8), pa1, o[1]); o[2] = mfma32(tr8<2>(vA + 8), pa1, o[2]); o[3] = mfma32(tr8<3>(vA + 8), pa1, o[3]);
      o[4] = mfma32(tr8<4>(vA + 8), pa1, o[4]); o[5] = mfma32(tr8<5>(vA + 8), pa1, o[5]); o[6] = mfma32(tr8<6>(vA + 8), pa1, o[6]); o[7] = mfma32(tr8<7>(vA + 8), pa1, o[7]);
      if (!hi8) {
#pragma unroll
        for (int r = 0; r < 4; ++r) {
          *(f4*)(red + 8 * r) = f4{o[0][r], o[1][r], o[2][r], o[3][r]};
          *(f4*)(red + 8 * r + 4) = f4{o[4][r], o[5][r], o[6][r], o[7][r]};
        }
      }
    }
  }
  BAR();
  STAMP(7);
  {
    const float* red = (const float*)(lds + L_RED) + ((w >> 2) * 64 + 2 * (w & 3) + (lane >> 5)) * RSTR + (lane & 31) * 4;
    f4 r = *(const f4*)red;
#pragma unroll
    for (int u = 1; u < 8; ++u) r += *(const f4*)(red + u * 8 * RSTR);
    stc(san(r), rws, op_off, WS_OP);
  }
  PIN();
  {
    const int og_off = ((br * 4 + (w >> 1)) * 32 + (ri >> 1)) * (16 * 512) + (lane >> 3) * 512 + (64 * (ri & 1) + 32 * (w & 1) + 4 * (lane & 7)) * 4;
    v4u g0 = ldc(rws, og_off, WS_OP), g1 = ldc(rws, og_off, WS_OP + 8 * 512);
    PIN();
    if (__builtin_expect(wave_bad(mx4(ml) == SENT), 0)) {
      unsigned spins = 0;
      do {
        __builtin_amdgcn_s_sleep(1);
        asm volatile("" ::: "memory");
        ml = ldc(rws, mg_off, WS_ML);
      } while (wave_bad(mx4(ml) == SENT) && ++spins < SPIN_CAP);
    }
    const f4 mlf = __builtin_bit_cast(f4, ml);
    const float M = max_x8_16_32(fmaxf(mlf[0], mlf[2]));
    float w0 = __builtin_amdgcn_exp2f(mlf[0] - M), w1 = __builtin_amdgcn_exp2f(mlf[2] - M);
    const float L = sum_x8_16_32(fmaf(w0, mlf[1], w1 * mlf[3]));
    const float inv = 1.0f / L;
    w0 *= inv; w1 *= inv;
    PIN();
    STAMP(8);
    tr64(wr, wt);
#pragma unroll
    for (int u = 0; u < 8; ++u) asm volatile("" : "+v"(wt[u]));
    PIN();
    for (unsigned spins = 0; wave_bad(max(mx4(g0), mx4(g1)) == SENT) && spins < SPIN_CAP; ++spins) {
      asm volatile("" ::: "memory");
      g0 = ldc(rws, og_off, WS_OP); g1 = ldc(rws, og_off, WS_OP + 8 * 512);
    }
    STAMP(9);
    const f4 v = __builtin_bit_cast(f4, g0) * w0 + __builtin_bit_cast(f4, g1) * w1;
    const float y0 = red32(v[0], v[2]), y1 = red32(v[1], v[3]);
    float y = red16(y0, y1);
    y = y + ror8(y);
    const __bf16 yb = (__bf16)y;
    *(__bf16*)(lds + L_O + ((w >> 1) * 64 + 32 * (w & 1) + 4 * (lane & 7) + 2 * (lane >> 5) + (G & 1)) * 2) = yb;
  }
  BAR();
  {
    const v4u a = *(const v4u*)(lds + L_O + ((c16 >> 2) * 64 + 8 * (2 * G + (c16 & 1))) * 2);
    stc(san(tile64(wt, a, hi8)), rws, p2_off, WS_P2);
  }
  STAMP(10);
  if (w == 0) {
    const int pg_off = ((br * 2 + ch) * 64 + ri) * (64 * 128) + lane * 16;
    v4u g[8];
    unsigned spins = 0;
    for (;;) {
      unsigned m = 0;
      asm volatile("" ::: "memory");
#pragma unroll
      for (int u = 0; u < 8; ++u) g[u] = ldc(rws, pg_off, WS_P2 + u * 1024);
#pragma unroll
      for (int u = 0; u < 8; ++u) m = max(m, mx4(g[u]));
      if (!wave_bad(m == SENT) || ++spins >= SPIN_CAP) break;
    }
    f4 v = __builtin_bit_cast(f4, g[0]);
#pragma unroll
    for (int u = 1; u < 8; ++u) v += __builtin_bit_cast(f4, g[u]);
    const float y0 = red32(v[0], v[2]), y1 = red32(v[1], v[3]);
    float y = red16(y0, y1);
    y = y + ror8(y);
    if (!hi8) {
      const int pc = lane & 7;
      ((__bf16*)p.out)[(pc >> 1) * 2048 + br * 1024 + 512 * ch + 8 * ri + 4 * (pc & 1) + 2 * (lane >> 5) + (G & 1)] = (__bf16)y;
    }
    if (j == 0 && lane == 0) __builtin_amdgcn_raw_buffer_store_b64(v2u{(unsigned)tag, (unsigned)(tag >> 32)}, rws, 0, WS_EPOCH, AUX_SC1);
  }
  STAMP(11);
}

extern "C" void kernel_launch(void* const* d_in, const int* in_sizes, int n_in, void* d_out, int out_size, void* d_ws, size_t ws_size,
                              hipStream_t stream) {
  Params p;
  p.xA = d_in[0]; p.kA = d_in[1]; p.vA = d_in[2]; p.wqA = d_in[3]; p.woA = d_in[4];
  p.xB = d_in[5]; p.kB = d_in[6]; p.vB = d_in[7]; p.wqB = d_in[8]; p.woB = d_in[9];
  p.out = d_out; p.ws = d_ws;
  fused_decode<<<NWG, NTHR, 0, stream>>>(p);
}
```

```cpp
#include <hip/hip_runtime.h>
#include <hip/hip_bf16.h>
#include <stdint.h>

typedef unsigned v4u __attribute__((ext_vector_type(4)));
typedef unsigned v2u __attribute__((ext_vector_type(2)));
typedef float f4 __attribute__((ext_vector_type(4)));
typedef __bf16 bf8 __attribute__((ext_vector_type(8)));
typedef short s4 __attribute__((ext_vector_type(4)));
typedef __amdgpu_buffer_rsrc_t rsrc_t;
typedef unsigned long long u64;

#define STAMPS 0

constexpr int NWG = 256, NTHR = 704;
constexpr int SK = 8192, WIN = 2048, DH = 128, DM = 1024, NQ = 4096;
constexpr unsigned WS_EPOCH = 0;
constexpr unsigned WS_QP = 4096;
constexpr unsigned WS_Q1 = WS_QP + 2 * 4 * 4096 * 4;
constexpr unsigned WS_ML = WS_Q1 + 256 * 4096;
constexpr unsigned WS_OP = WS_ML + 2 * 16 * 16 * 128;
constexpr unsigned WS_P2 = WS_OP + 2 * 4 * 32 * 16 * 512;
constexpr unsigned WS_END = WS_P2 + 2 * 2 * 64 * 64 * 128;
constexpr unsigned WS_STAMP = WS_END;
#define AUXS 16
#define AUXL 16
constexpr int AUX_NT = 2, AUX_SC1 = 16;
constexpr unsigned SPIN_CAP = 1u << 14, LSPIN_CAP = 1u << 18;

struct Params {
  const void *xA, *kA, *vA, *wqA, *woA, *xB, *kB, *vB, *wqB, *woB;
  void *out, *ws;
};

__device__ __forceinline__ rsrc_t mk(const void* p, unsigned bytes) { return __builtin_amdgcn_make_buffer_rsrc((void*)p, 0, (int)bytes, 0x00020000); }
__device__ __forceinline__ v4u ldnt(rsrc_t r, int voff, int soff) { return __builtin_amdgcn_raw_buffer_load_b128(r, voff, soff, AUX_NT); }
__device__ __forceinline__ v4u ldc(rsrc_t r, int voff, int soff) { return __builtin_amdgcn_raw_buffer_load_b128(r, voff, soff, AUXL); }
__device__ __forceinline__ void stc(v4u v, rsrc_t r, int voff, int soff) { __builtin_amdgcn_raw_buffer_store_b128(v, r, voff, soff, AUXS); }
__device__ __forceinline__ f4 mfma32(v4u a, v4u b, f4 c) { return __builtin_amdgcn_mfma_f32_16x16x32_bf16(__builtin_bit_cast(bf8, a), __builtin_bit_cast(bf8, b), c, 0, 0, 0); }
__device__ __forceinline__ f4 mfma16(v2u a, v2u b, f4 c) { return __builtin_amdgcn_mfma_f32_16x16x16bf16_1k(__builtin_bit_cast(s4, a), __builtin_bit_cast(s4, b), c, 0, 0, 0); }
__device__ __forceinline__ unsigned fu(float x) { return __builtin_bit_cast(unsigned, x); }
__device__ __forceinline__ float uf(unsigned x) { return __builtin_bit_cast(float, x); }
__device__ __forceinline__ unsigned ror8u(unsigned x) { return (unsigned)__builtin_amdgcn_mov_dpp((int)x, 0x128, 0xf, 0xf, true); }
__device__ __forceinline__ float ror8(float x) { return uf(ror8u(fu(x))); }
__device__ __forceinline__ unsigned pk2(float lo, float hi) {
  typedef __bf16 bf2 __attribute__((ext_vector_type(2)));
  typedef float f2 __attribute__((ext_vector_type(2)));
  f2 v = {lo, hi};
  return __builtin_bit_cast(unsigned, __builtin_convertvector(v, bf2));
}
template <int T> __device__ __forceinline__ unsigned tr(const v4u& a, const v4u& b) {
  return __builtin_amdgcn_perm(b[T >> 1], a[T >> 1], (T & 1) ? 0x07060302u : 0x05040100u);
}
template <int T> __device__ __forceinline__ v4u tr8(const v4u* r) { return v4u{tr<T>(r[0], r[1]), tr<T>(r[2], r[3]), tr<T>(r[4], r[5]), tr<T>(r[6], r[7])}; }
template <int T> __device__ __forceinline__ v2u tr4(const v4u* r) { return v2u{tr<T>(r[0], r[1]), tr<T>(r[2], r[3])}; }
__device__ __forceinline__ float red32(float a, float b) { auto s = __builtin_amdgcn_permlane32_swap(fu(a), fu(b), false, false); return uf(s[0]) + uf(s[1]); }
__device__ __forceinline__ float red16(float a, float b) { auto s = __builtin_amdgcn_permlane16_swap(fu(a), fu(b), false, false); return uf(s[0]) + uf(s[1]); }
__device__ __forceinline__ float max_x8_16_32(float x) {
  x = fmaxf(x, ror8(x));
  auto s = __builtin_amdgcn_permlane16_swap(fu(x), fu(x), false, false); x = fmaxf(uf(s[0]), uf(s[1]));
  auto t = __builtin_amdgcn_permlane32_swap(fu(x), fu(x), false, false); return fmaxf(uf(t[0]), uf(t[1]));
}
__device__ __forceinline__ float sum_x8_16_32(float x) { x = x + ror8(x); x = red16(x, x); return red32(x, x); }
__device__ __forceinline__ v4u mark(f4 v, unsigned tf) {
  const v4u u = __builtin_bit_cast(v4u, v);
  return v4u{(u[0] & ~15u) | (tf & 15u), (u[1] & ~15u) | ((tf >> 4) & 15u), (u[2] & ~15u) | ((tf >> 8) & 15u), (u[3] & ~15u) | ((tf >> 12) & 15u)};
}
__device__ __forceinline__ unsigned mis(const v4u& g, unsigned tf) { return ((g[0] ^ tf) | (g[1] ^ (tf >> 4)) | (g[2] ^ (tf >> 8)) | (g[3] ^ (tf >> 12))) & 15u; }
__device__ __forceinline__ f4 val(const v4u& g) { return __builtin_bit_cast(f4, g & ~15u); }
__device__ __forceinline__ bool wave_bad(bool bad) { return __builtin_amdgcn_ballot_w64(bad) != 0ull; }
#define PIN() __builtin_amdgcn_sched_barrier(0)
#define VMW(n) asm volatile("s_waitcnt vmcnt(" #n ")" ::: "memory")
#define BAR() do { asm volatile("s_waitcnt lgkmcnt(0)" ::: "memory"); __builtin_amdgcn_s_barrier(); asm volatile("" ::: "memory"); } while (0)

__device__ __forceinline__ void tr64(const v4u* wr, v4u* wt) {
  wt[0] = tr8<0>(wr); wt[1] = tr8<1>(wr); wt[2] = tr8<2>(wr); wt[3] = tr8<3>(wr);
  wt[4] = tr8<4>(wr); wt[5] = tr8<5>(wr); wt[6] = tr8<6>(wr); wt[7] = tr8<7>(wr);
}
__device__ __forceinline__ void tile64_acc(const v4u* wt, v4u a, f4* d) {
#pragma unroll
  for (int t = 0; t < 8; ++t) d[t] = mfma32(a, wt[t], d[t]);
}
__device__ __forceinline__ float sel(bool c, float a, float b) { asm volatile("" : "+v"(a), "+v"(b)); return c ? a : b; }
__device__ __forceinline__ f4 tile64_fin(const f4* d, bool hi8) {
  f4 r;
#pragma unroll
  for (int u = 0; u < 4; ++u) r[u] = sel(hi8, d[4 + u][1], d[u][0]) + ror8(sel(hi8, d[u][1], d[4 + u][0]));
  return r;
}
__device__ __forceinline__ void repoll(v4u& g, unsigned tf, int lane, rsrc_t r, int voff, int soff) {
  const bool bad = ((__builtin_amdgcn_ballot_w64(mis(g, tf) != 0u) >> (lane & 56)) & 0xffull) != 0ull;
  const v4u n = ldc(r, bad ? voff : 0x7ffffff0, soff);
  g = v4u{bad ? n[0] : g[0], bad ? n[1] : g[1], bad ? n[2] : g[2], bad ? n[3] : g[3]};
}
template <int N, int SLEEP> __device__ __forceinline__ void gather2(v4u* g, unsigned tf, int lane, rsrc_t r, int voff, int soff, int stride) {
#pragma unroll
  for (int u = 0; u < N; ++u) g[u] = ldc(r, voff, soff + (u & 3) * 1024 + (u >> 2) * stride);
#pragma unroll 1
  for (unsigned spins = 0; spins < SPIN_CAP; ++spins) {
    unsigned m = 0;
#pragma unroll
    for (int u = 0; u < N; ++u) m |= mis(g[u], tf);
    if (!wave_bad(m != 0u)) break;
    if (SLEEP) __builtin_amdgcn_s_sleep(SLEEP);
    asm volatile("" ::: "memory");
#pragma unroll
    for (int u = 0; u < N; ++u) repoll(g[u], tf, lane, r, voff, soff + (u & 3) * 1024 + (u >> 2) * stride);
  }
}
__device__ __forceinline__ u64 launch_tag(const void* ws) {
  u64 e;
  asm volatile("s_load_dwordx2 %0, %1, 0x0\n\ts_waitcnt lgkmcnt(0)" : "=s"(e) : "s"(ws) : "memory");
  u64 d = (e + 0x9E3779B97F4A7C15ull) * 0xBF58476D1CE4E5B9ull;
  d ^= d >> 29;
  return e ^ (d | 0x1111111111111111ull);
}
__device__ __forceinline__ void pv32(const v4u* vr, v4u pf, f4* o) {
  o[0] = mfma32(tr8<0>(vr), pf, o[0]); o[1] = mfma32(tr8<1>(vr), pf, o[1]); o[2] = mfma32(tr8<2>(vr), pf, o[2]); o[3] = mfma32(tr8<3>(vr), pf, o[3]);
  o[4] = mfma32(tr8<4>(vr), pf, o[4]); o[5] = mfma32(tr8<5>(vr), pf, o[5]); o[6] = mfma32(tr8<6>(vr), pf, o[6]); o[7] = mfma32(tr8<7>(vr), pf, o[7]);
}
__device__ __forceinline__ void put_o(float* red, const f4* o) {
#pragma unroll
  for (int r = 0; r < 4; ++r) {
    *(f4*)(red + 8 * r) = f4{o[0][r], o[1][r], o[2][r], o[3][r]};
    *(f4*)(red + 8 * r + 4) = f4{o[4][r], o[5][r], o[6][r], o[7][r]};
  }
}
__device__ __forceinline__ void qk16(const v4u* kr, v4u q0, v4u q1, bool t0lane, float& z0, float& z1) {
  const f4 z = {0.f, 0.f, 0.f, 0.f};
  f4 da = mfma32(kr[1], q1, mfma32(kr[0], q0, z));
  f4 db = mfma32(kr[3], q1, mfma32(kr[2], q0, z));
  const float x0 = t0lane ? da[0] : db[0], x1 = t0lane ? da[1] : db[1], x2 = t0lane ? da[2] : db[2], x3 = t0lane ? da[3] : db[3];
  auto s = __builtin_amdgcn_permlane32_swap(fu(x0), fu(x1), false, false);
  auto t = __builtin_amdgcn_permlane32_swap(fu(x2), fu(x3), false, false);
  z0 = uf(s[0]) + ror8(uf(s[1]));
  z1 = uf(t[0]) + ror8(uf(t[1]));
}

constexpr int NSLOT = 7;
constexpr int RSTR = 132;
constexpr int L_RING = 0;
constexpr int L_RED = L_RING + 2 * NSLOT * 8192;
constexpr int L_Q = L_RED + 8 * 8 * RSTR * 4;
constexpr int QSTR = 272;
constexpr int L_ML = L_Q + 2 * 8 * QSTR;
constexpr int L_O = L_ML + 2 * 8 * 8 * 8;
constexpr int L_WT = L_O + 512;
constexpr int L_CTL = L_WT + 256;
constexpr int L_END = L_CTL + 128;
enum { C_DONE = 0, C_FULL = 8, C_QR = 10, C_Q = 11, C_ML = 12, C_OB = 13, C_OBD = 14, C_OA = 15, C_WT = 16, C_O = 17, C_P2 = 18 };
typedef __attribute__((address_space(3))) unsigned lds_u32;
#define CB() asm volatile("" ::: "memory")
__device__ __forceinline__ void arrive(unsigned char* lds, int c, int lane) {
  CB();
  if (lane == 0) __hip_atomic_fetch_add((lds_u32*)(lds + L_CTL) + c, 1u, __ATOMIC_RELAXED, __HIP_MEMORY_SCOPE_WORKGROUP);
  CB();
}
__device__ __forceinline__ void setw(unsigned char* lds, int c, unsigned v) {
  CB();
  __hip_atomic_store((lds_u32*)(lds + L_CTL) + c, v, __ATOMIC_RELAXED, __HIP_MEMORY_SCOPE_WORKGROUP);
  CB();
}
template <int SLEEP> __device__ __forceinline__ void await(unsigned char* lds, int c, unsigned n) {
  CB();
#pragma unroll 1
  for (unsigned spins = 0; spins < LSPIN_CAP; ++spins) {
    if ((unsigned)__builtin_amdgcn_readfirstlane(__hip_atomic_load((lds_u32*)(lds + L_CTL) + c, __ATOMIC_RELAXED, __HIP_MEMORY_SCOPE_WORKGROUP)) >= n) break;
    if (SLEEP) __builtin_amdgcn_s_sleep(SLEEP);
  }
  CB();
}

__device__ __forceinline__ void until(u64 t0, unsigned ticks) {
#pragma unroll 1
  while ((unsigned)(__builtin_amdgcn_s_memrealtime() - t0) < ticks) __builtin_amdgcn_s_sleep(2);
}

constexpr int NSTAMP = 16;
#define STAMP(i)
#define STAMP_OUT()

#define T1 580
#define T2 850
#define DBG 0
#define PRE2 1
#define FM 22
#define S1 10
#define S4 1
#define HOP1EARLY 0
#define WND 12
#define RELOAD() do { __builtin_amdgcn_s_sleep(1); CB(); } while (0)
typedef int v4i __attribute__((ext_vector_type(4)));
__device__ __forceinline__ v4i srd(const void* p, unsigned bytes) { const u64 a = (u64)p; return v4i{(int)(unsigned)a, (int)(unsigned)(a >> 32) & 0xffff, (int)bytes, 0x00020000}; }
__device__ __forceinline__ void dma(int m0, int voff, v4i r, int soff) {
  asm volatile("s_mov_b32 m0, %0\n\ts_nop 0\n\tbuffer_load_dwordx4 %1, %2, %3 offen nt lds" :: "s"(m0), "v"(voff), "s"(r), "s"(soff) : "memory");
}
template <int N> __device__ __forceinline__ void landed(int full_addr, int cnt) {
  asm volatile("s_waitcnt vmcnt(%2)\n\tds_write_b32 %0, %1" :: "v"(full_addr), "v"(cnt), "i"(N) : "memory");
}
template <int N, int LO> __device__ __forceinline__ void drain2(int full_addr, int total, int& pub) {
  if constexpr (N >= LO) {
    pub = max(total - N, pub);
    landed<N>(full_addr, pub);
    drain2<N - 2, LO>(full_addr, total, pub);
  }
}
template <int V> struct IC { static constexpr int value = V; };

__global__ void __launch_bounds__(NTHR, 3) fused_decode(Params p) {
  __shared__ __attribute__((aligned(16))) unsigned char lds[L_END];
  const int tid = threadIdx.x, lane = tid & 63, wv = __builtin_amdgcn_readfirstlane(tid >> 6);
  const int j = blockIdx.x;
  const int G = lane >> 4, c16 = lane & 15;
  const bool hi8 = (lane & 8) != 0;
  const int pair = j >> 4, b = pair >> 2, hkv = pair & 3, s = j & 15;
  const int br = j >> 7, cb = (j & 127) >> 3, ri = (j & 127) >> 1, ch = j & 1;
  const unsigned KVB = 16u * SK * DH * 2, WB = DM * NQ * 2;

  if (__builtin_expect(wv < 2, 1)) {
    __builtin_amdgcn_s_setprio(3);
    const int l = wv;
    const int rot = j * 5 + (j >> 3);
    const int lin = lane * 16;
    const int vk0 = G * 256 + ((c16 ^ (2 * G)) * 16), vk1 = G * 256 + ((c16 ^ (8 + 2 * G)) * 16);
    const int voq = (8 * (lane >> 3)) * (NQ * 2) + (lane & 7) * 16, voo = (8 * (lane >> 3)) * (DM * 2) + (lane & 7) * 16;
    const int lds0 = (int)(unsigned)(size_t)(__attribute__((address_space(3))) unsigned char*)lds;
    const int ring = lds0 + L_RING + l * (NSLOT * 8192), full_addr = lds0 + L_CTL + (C_FULL + l) * 4, done_addr = lds0 + L_CTL + (C_DONE + 4 * l) * 4;
    int f = 0, slot = 0, pub = 0, freed = NSLOT;
    auto fill = [&](auto wc, v4i r, int v0, int v1, int base, int sc, int sh) {
      constexpr int W = decltype(wc)::value;
      if (f >= freed) {
        pub = 8 * f;
        landed<0>(full_addr, pub);
#pragma unroll 1
        for (unsigned spins = 0; spins < LSPIN_CAP; ++spins) {
          v4i d;
          asm volatile("ds_read_b128 %0, %1\n\ts_waitcnt lgkmcnt(0)" : "=v"(d) : "v"(done_addr) : "memory");
          freed = NSLOT + __builtin_amdgcn_readfirstlane(min(min(d[0], d[1]), min(d[2], d[3])));
          if (f < freed) break;
          __builtin_amdgcn_s_sleep(1);
        }
      } else if (f + 2 >= freed) {
        v4i d;
        asm volatile("ds_read_b128 %0, %1\n\ts_waitcnt lgkmcnt(0)" : "=v"(d) : "v"(done_addr) : "memory");
        freed = NSLOT + __builtin_amdgcn_readfirstlane(min(min(d[0], d[1]), min(d[2], d[3])));
      }
      const int dst = ring + slot * 8192;
#pragma unroll
      for (int c = 0; c < 4; ++c) {
        if constexpr (W > 0) { pub = max(8 * f + 2 * c - (W - 2), pub); landed<W - 2>(full_addr, pub); }
        dma(dst + c * 2048, v0, r, base + c * sc);
        dma(dst + c * 2048 + 1024, v1, r, base + c * sc + sh);
      }
      ++f; slot = slot == NSLOT - 1 ? 0 : slot + 1;
    };
    const int baseA = (pair * SK + s * 512 + l * 32) * 256, baseB = (pair * SK + (SK - WIN) + s * 128 + l * 32) * 256;
    {
      const v4i r = srd(br ? p.wqB : p.wqA, WB);
      const int baseQ = (128 * (j & 7) + 64 * l) * (NQ * 2) + cb * 512;
      fill(IC<0>{}, r, voq, voq, baseQ, 128, NQ * 2);
      __builtin_amdgcn_s_barrier();
#pragma unroll 1
      for (int t = 1; t < 4; ++t) fill(IC<WND>{}, r, voq, voq, baseQ + 2 * t * (NQ * 2), 128, NQ * 2);
    }
    STAMP(1);
    {
      const v4i r = srd(p.kA, KVB);
#pragma unroll 1
      for (int t = 0; t < 8; ++t) fill(IC<WND>{}, r, vk0, vk1, baseA + (((t + rot) & 7) << 14), 2048, 1024);
    }
    {
      const v4i r = srd(p.kB, KVB);
#pragma unroll 1
      for (int t = 0; t < 2; ++t) fill(IC<WND>{}, r, vk0, vk1, baseB + (t << 14), 2048, 1024);
    }
    STAMP(2);
    {
      const v4i r = srd(p.vB, KVB);
#pragma unroll 1
      for (int t = 0; t < 2; ++t) fill(IC<WND>{}, r, lin, lin, baseB + (t << 14), 2048, 1024);
    }
    {
      const v4i r = srd(p.vA, KVB);
#pragma unroll 1
      for (int t = 0; t < 8; ++t) fill(IC<WND>{}, r, lin, lin, baseA + (((t + rot) & 7) << 14), 2048, 1024);
    }
    STAMP(3);
    {
      const v4i r = srd(br ? p.woB : p.woA, WB);
      const int baseO = (64 * ri) * (DM * 2) + (512 * ch + 256 * l) * 2;
#pragma unroll 1
      for (int t = 0; t < 4; ++t) fill(IC<WND>{}, r, voo, voo, baseO + 2 * t * (DM * 2), 128, DM * 2);
    }
    STAMP(4);
    drain2<WND - 2, 0>(full_addr, 8 * 28, pub);
    STAMP(5);
    STAMP_OUT();
    return;
  }

  const rsrc_t rws = mk(p.ws, WS_END);
  const f4 z = {0.f, 0.f, 0.f, 0.f};
  if (wv == 2) {
    __builtin_amdgcn_s_setprio(2);
    const u64 t0 = __builtin_amdgcn_s_memrealtime();
    if (lane < 32) *(unsigned*)(lds + L_CTL + lane * 4) = 0u;
    asm volatile("s_waitcnt lgkmcnt(0)" ::: "memory");
    __builtin_amdgcn_s_barrier();
    const u64 tag = launch_tag(p.ws);
    const unsigned tl = (unsigned)(tag >> (16 * (lane & 3)));
    await<2>(lds, C_QR, 8);
    {
      const int un = 2 * (lane & 3) + (hi8 ? 1 : 0);
      const unsigned tu = (unsigned)(tag >> (16 * (un & 3)));
#pragma unroll
      for (int u = 0; u < 4; ++u)
        stc(mark(*(const f4*)(lds + L_RED + u * 1024 + lane * 16) + *(const f4*)(lds + L_RED + (u + 4) * 1024 + lane * 16), tu), rws,
            ((lane >> 2) & 1) * 4096 + G * 128 + un * 16, WS_Q1 + ((j & ~7) + 2 * u) * 4096 + (j & 7) * 512);
    }
    STAMP(3);
    until(t0, T1);
    {
      v4u g[4];
      gather2<4, S1>(g, tl, lane, rws, lane * 16, WS_Q1 + j * 4096, 0);
      const f4 r = (val(g[0]) + val(g[1])) + (val(g[2]) + val(g[3]));
      const unsigned r01 = pk2(red32(r[0], r[0]), red32(r[1], r[1])), r23 = pk2(red32(r[2], r[2]), red32(r[3], r[3]));
      if (lane < 32)
        stc(mark(f4{uf(r01 << 16), uf(r01 & 0xffff0000u), uf(r23 << 16), uf(r23 & 0xffff0000u)}, tl), rws,
            (lane >> 3) * (NQ * 4) + (lane & 7) * 16, WS_QP + (br * 4 * NQ + 32 * (j & 127)) * 4);
    }
    STAMP(4);
    until(t0, T2);
    {
      v4u g[8];
      gather2<8, S1>(g, tl, lane, rws, lane * 16, WS_QP + (b * NQ + hkv * 1024) * 4, 4 * NQ * 4);
#pragma unroll
      for (int u = 0; u < 8; ++u)
        *(v2u*)(lds + L_Q + (2 * u + (lane >> 5)) * QSTR + (lane & 31) * 8) =
            v2u{__builtin_amdgcn_perm(g[u][1], g[u][0], 0x07060302u), __builtin_amdgcn_perm(g[u][3], g[u][2], 0x07060302u)};
    }
    setw(lds, C_Q, 1u);
    STAMP(5);
    await<2>(lds, C_ML, 8);
    {
      f4 mv[4];
#pragma unroll
      for (int u = 0; u < 4; ++u) mv[u] = *(const f4*)(lds + L_ML + c16 * 64 + u * 16);
      float M = -INFINITY, L = 0.f;
#pragma unroll
      for (int u = 0; u < 4; ++u) M = fmaxf(M, fmaxf(mv[u][0], mv[u][2]));
#pragma unroll
      for (int u = 0; u < 4; ++u) { L = fmaf(__builtin_amdgcn_exp2f(mv[u][0] - M), mv[u][1], L); L = fmaf(__builtin_amdgcn_exp2f(mv[u][2] - M), mv[u][3], L); }
      if (lane < 16) stc(v4u{fu(M), (unsigned)tag, fu(L), (unsigned)(tag >> 32)}, rws, (hi8 ? 256 * 128 : 0) + (lane & 7) * 16, WS_ML + j * 128);
    }
    STAMP(6);
    STAMP(7);
    await<4>(lds, C_FULL, 8 * FM);
    {
      const int mo = (G * 4 * 16 + c16) * 128, mg = WS_ML + ((br * 16 + (ri >> 4)) * 16) * 128 + ((ri >> 1) & 7) * 16;
      v4u ml = ldc(rws, mo, mg);
#pragma unroll 1
      for (unsigned spins = 0; spins < SPIN_CAP; ++spins) {
        const bool bad = ml[1] != (unsigned)tag || ml[3] != (unsigned)(tag >> 32);
        if (!wave_bad(bad)) break;
        __builtin_amdgcn_s_sleep(S1); CB();
        const v4u n = ldc(rws, bad ? mo : 0x7ffffff0, mg);
        ml = v4u{bad ? n[0] : ml[0], bad ? n[1] : ml[1], bad ? n[2] : ml[2], bad ? n[3] : ml[3]};
      }
      const float m = uf(ml[0]);
      float M = m;
      M = fmaxf(M, __shfl_xor(M, 1)); M = fmaxf(M, __shfl_xor(M, 2)); M = fmaxf(M, __shfl_xor(M, 4)); M = fmaxf(M, ror8(M));
      const float w0 = __builtin_amdgcn_exp2f(m - M);
      float L = w0 * uf(ml[2]);
      L += __shfl_xor(L, 1); L += __shfl_xor(L, 2); L += __shfl_xor(L, 4); L += ror8(L);
      *(float*)(lds + L_WT + (G * 8 + (lane & 7)) * 8 + (hi8 ? 4 : 0)) = w0 / L;
      setw(lds, C_WT, 1u);
    }
    STAMP(8);
    await<1>(lds, C_P2, 8);
    STAMP(9);
    {
      v4u g[8];
      gather2<8, 0>(g, tl, lane, rws, lane * 16, WS_P2 + ((br * 2 + ch) * 64 + ri) * (64 * 128), 4096);
      f4 v = val(g[0]);
#pragma unroll
      for (int u = 1; u < 8; ++u) v += val(g[u]);
      const float y0 = red32(v[0], v[2]), y1 = red32(v[1], v[3]);
      float y = red16(y0, y1);
      y = y + ror8(y);
      if (!hi8) {
        const int pc = lane & 7;
        ((__bf16*)p.out)[(pc >> 1) * 2048 + br * 1024 + 512 * ch + 8 * ri + 4 * (pc & 1) + 2 * (lane >> 5) + (G & 1)] = (__bf16)y;
      }
      if (j == 0 && lane == 0) __builtin_amdgcn_raw_buffer_store_b64(v2u{(unsigned)tag, (unsigned)(tag >> 32)}, rws, 0, WS_EPOCH, AUX_SC1);
    }
    STAMP(10);
    STAMP_OUT();
    return;
  }

  const int w = wv - 3;
  const v4u xa = __builtin_amdgcn_raw_buffer_load_b128(mk(br ? p.xB : p.xA, 4 * DM * 2), ((c16 >> 2) * DM + 8 * (2 * G + (c16 & 1))) * 2,
                                                       (128 * (j & 7) + 64 * (w >> 2)) * 2, 0);
  const unsigned char* mine = lds + L_RING + (w >> 2) * (NSLOT * 8192) + (w & 3) * 2048;
  const int kr0 = (lane & 7) * 256 + (((hi8 ? 4 : 0) + G) ^ (2 * (lane & 7))) * 16;
  const int vr0 = (4 * (G & 1) + (G >> 1)) * 256 + c16 * 16;
  __builtin_amdgcn_s_barrier();
  const u64 tag = launch_tag(p.ws);
#define RD(f, o) (*(const v4u*)(mine + ((f) % NSLOT) * 8192 + (o)))
#define TAKE(f, N, dst, T, O0, O1, SL) do { await<SL>(lds, C_FULL + (w >> 2), 8 * ((f) + (N) - 1) + 2 * (w & 3) + 2); \
    _Pragma("unroll") for (int u_ = 0; u_ < (N); ++u_) { const int t_ = (T) + u_; (void)t_; dst[2 * t_] = RD((f) + u_, O0); dst[2 * t_ + 1] = RD((f) + u_, O1); } \
    asm volatile("s_waitcnt lgkmcnt(0)" ::: "memory"); setw(lds, C_DONE + w, (f) + (N)); \
    _Pragma("unroll") for (int u_ = 0; u_ < 2 * (N); ++u_) asm volatile("" : "+v"(dst[2 * (T) + u_])); } while (0)
#define TAKEK(f, N, dst, T) TAKE(f, N, dst, T, kr0 ^ ((t_ & 1) * 64), kr0 ^ ((t_ & 1) * 64) ^ 128, 2)
#define TAKEV(f, N, dst, T, SL) TAKE(f, N, dst, T, vr0, vr0 + 512, SL)
#define TAKEW(f, N, dst, T, SL) TAKE(f, N, dst, T, lane * 16, lane * 16 + 1024, SL)
  v4u kB[4], vB[4], kA[16], vt[8], wt[8];
  {
    f4 d[8] = {z, z, z, z, z, z, z, z};
    TAKEW(0, 4, vt, 0, 1);
    tr64(vt, wt); tile64_acc(wt, xa, d);
    *(f4*)(lds + L_RED + w * 1024 + lane * 16) = tile64_fin(d, hi8);
    arrive(lds, C_QR, lane);
  }
  STAMP(1);
  TAKEK(4, 2, kA, 0); TAKEK(6, 2, kA, 2); TAKEK(8, 2, kA, 4); TAKEK(10, 2, kA, 6); TAKEK(12, 2, kB, 0);
  TAKEV(14, 2, vB, 0, 2);
  TAKEV(16, 2, vt, 0, 2);
  STAMP(2);
  await<2>(lds, C_Q, 1);
  STAMP(3);
  constexpr float C2 = 0.088388347648318440f * 1.4426950408889634f;
  unsigned pA[4], pB;
  float* red = (float*)(lds + L_RED) + (w * 8 + (lane & 7)) * RSTR + 32 * G;
  {
    const unsigned char* qp = lds + L_Q + (lane & 7) * QSTR + (hi8 ? 64 : 0) + G * 16;
    const bool t0lane = (G >> 1) == (c16 >> 3);
    float za[8], zb[2];
    const v4u qa0 = *(const v4u*)qp, qa1 = *(const v4u*)(qp + 128), qb0 = *(const v4u*)(qp + 8 * QSTR), qb1 = *(const v4u*)(qp + 8 * QSTR + 128);
#pragma unroll
    for (int u = 0; u < 4; ++u) qk16(kA + 4 * u, qa0, qa1, t0lane, za[2 * u], za[2 * u + 1]);
    qk16(kB, qb0, qb1, t0lane, zb[0], zb[1]);
    float mA = fmaxf(fmaxf(fmaxf(za[0], za[1]), fmaxf(za[2], za[3])), fmaxf(fmaxf(za[4], za[5]), fmaxf(za[6], za[7])));
    mA = max_x8_16_32(mA) * C2;
    const float mB = max_x8_16_32(fmaxf(zb[0], zb[1])) * C2;
    float lA = 0.f;
#pragma unroll
    for (int u = 0; u < 8; ++u) { za[u] = __builtin_amdgcn_exp2f(fmaf(za[u], C2, -mA)); lA += za[u]; }
    zb[0] = __builtin_amdgcn_exp2f(fmaf(zb[0], C2, -mB)); zb[1] = __builtin_amdgcn_exp2f(fmaf(zb[1], C2, -mB));
    lA = sum_x8_16_32(lA);
    const float lB = sum_x8_16_32(zb[0] + zb[1]);
    if (lane < 16) *(float2*)(lds + L_ML + (lane * 8 + w) * 8) = hi8 ? make_float2(mB, lB) : make_float2(mA, lA);
    arrive(lds, C_ML, lane);
    await<1>(lds, C_ML, 8);
    float MA = -INFINITY, MB = -INFINITY;
#pragma unroll
    for (int u = 0; u < 4; ++u) {
      const f4 ma = *(const f4*)(lds + L_ML + (lane & 7) * 64 + u * 16), mb = *(const f4*)(lds + L_ML + (8 + (lane & 7)) * 64 + u * 16);
      MA = fmaxf(MA, fmaxf(ma[0], ma[2])); MB = fmaxf(MB, fmaxf(mb[0], mb[2]));
    }
    const float fA = __builtin_amdgcn_exp2f(mA - MA), fB = __builtin_amdgcn_exp2f(mB - MB);
#pragma unroll
    for (int u = 0; u < 4; ++u) pA[u] = pk2(za[2 * u] * fA, za[2 * u + 1] * fA);
    pB = pk2(zb[0] * fB, zb[1] * fB);
  }
  STAMP(4);
  const unsigned tl = (unsigned)(tag >> (16 * (lane & 3)));
  const float* rd = (const float*)(lds + L_RED) + (2 * (w & 3) + (lane >> 5)) * RSTR + (lane & 31) * 4;
  const int opv = (lane >> 5) * (16 * 512) + (lane & 31) * 16, ops = WS_OP + (((w >> 2) * 4 + b) * 32 + hkv * 8 + 2 * (w & 3)) * (16 * 512) + s * 512;
  {
    const v2u pb = {pB, ror8u(pB)};
    f4 o[8];
    o[0] = mfma16(tr4<0>(vB), pb, z); o[1] = mfma16(tr4<1>(vB), pb, z); o[2] = mfma16(tr4<2>(vB), pb, z); o[3] = mfma16(tr4<3>(vB), pb, z);
    o[4] = mfma16(tr4<4>(vB), pb, z); o[5] = mfma16(tr4<5>(vB), pb, z); o[6] = mfma16(tr4<6>(vB), pb, z); o[7] = mfma16(tr4<7>(vB), pb, z);
    if (!hi8) put_o(red, o);
    arrive(lds, C_OB, lane);
  }
  if (w >= 4) {
    await<1>(lds, C_OB, 8);
    f4 r = *(const f4*)rd;
#pragma unroll
    for (int u = 1; u < 8; ++u) r += *(const f4*)(rd + u * 8 * RSTR);
    stc(mark(r, tl), rws, opv, ops);
    arrive(lds, C_OBD, lane);
  }
  TAKEV(18, 2, vt, 2, 1);
  f4 o[8] = {z, z, z, z, z, z, z, z};
  pv32(vt, v4u{pA[0], ror8u(pA[0]), pA[1], ror8u(pA[1])}, o);
#pragma unroll
  for (int u = 0; u < 8; ++u) asm volatile("" : "+v"(o[u]));
  STAMP(5);
  TAKEV(20, 3, vt, 0, 1);
  await<1>(lds, C_OBD, 4);
  TAKEV(23, 1, vt, 3, 0);
  STAMP(6);
  pv32(vt, v4u{pA[2], ror8u(pA[2]), pA[3], ror8u(pA[3])}, o);
  if (!hi8) put_o(red, o);
  arrive(lds, C_OA, lane);
  if (w < 4) {
    await<0>(lds, C_OA, 8);
    f4 r = *(const f4*)rd;
#pragma unroll
    for (int u = 1; u < 8; ++u) r += *(const f4*)(rd + u * 8 * RSTR);
    stc(mark(r, tl), rws, opv, ops);
  }
  STAMP(7);
  TAKEW(24, 2, vt, 0, 1);
  TAKEW(26, 2, vt, 2, 0);
  {
    const int og = WS_OP + ((br * 4 + (w >> 1)) * 32 + (ri >> 1)) * (16 * 512) + (64 * (ri & 1) + 32 * (w & 1)) * 4;
    const int ov = (lane >> 3) * 512 + (lane & 7) * 16, o1 = 8 * 512;
    v4u g0 = ldc(rws, ov, og), g1 = ldc(rws, ov, og + o1);
    PIN();
    STAMP(8);
    tr64(vt, wt);
#pragma unroll
    for (int u = 0; u < 8; ++u) asm volatile("" : "+v"(wt[u]));
    await<1>(lds, C_WT, 1);
    const float2 wgt = *(const float2*)(lds + L_WT + ((w >> 1) * 8 + (lane >> 3)) * 8);
    PIN();
#pragma unroll 1
    for (unsigned spins = 0; spins < SPIN_CAP; ++spins) {
      if (!wave_bad((mis(g0, tl) | mis(g1, tl)) != 0u)) break;
      __builtin_amdgcn_s_sleep(S4); CB();
      repoll(g0, tl, lane, rws, ov, og);
      repoll(g1, tl, lane, rws, ov, og + o1);
    }
    STAMP(9);
    const f4 v = val(g0) * wgt.x + val(g1) * wgt.y;
    const float y0 = red32(v[0], v[2]), y1 = red32(v[1], v[3]);
    float y = red16(y0, y1);
    y = y + ror8(y);
    *(__bf16*)(lds + L_O + ((w >> 1) * 64 + 32 * (w & 1) + 4 * (lane & 7) + 2 * (lane >> 5) + (G & 1)) * 2) = (__bf16)y;
  }
  arrive(lds, C_O, lane);
  await<0>(lds, C_O, 8);
  {
    const v4u a = *(const v4u*)(lds + L_O + ((c16 >> 2) * 64 + 8 * (2 * G + (c16 & 1))) * 2);
    f4 d[8] = {z, z, z, z, z, z, z, z};
    tile64_acc(wt, a, d);
    stc(mark(tile64_fin(d, hi8), (unsigned)(tag >> (16 * ((2 * G + (hi8 ? 1 : 0)) & 3)))), rws, (lane & 7) * (64 * 128) + G * 32 + (hi8 ? 16 : 0),
        WS_P2 + (((br * 2 + ch) * 64 + 8 * w) * 64 + ri) * 128);
  }
  arrive(lds, C_P2, lane);
  STAMP(10);
  STAMP_OUT();
}

extern "C" void kernel_launch(void* const* d_in, const int* in_sizes, int n_in, void* d_out, int out_size, void* d_ws, size_t ws_size,
                              hipStream_t stream) {
  Params p;
  p.xA = d_in[0]; p.kA = d_in[1]; p.vA = d_in[2]; p.wqA = d_in[3]; p.woA = d_in[4];
  p.xB = d_in[5]; p.kB = d_in[6]; p.vB = d_in[7]; p.wqB = d_in[8]; p.woB = d_in[9];
  p.out = d_out; p.ws = d_ws;
  fused_decode<<<NWG, NTHR, 0, stream>>>(p);
}
```

```cpp
#define HOLDL 2
#include <hip/hip_runtime.h>
#include <hip/hip_bf16.h>
#include <stdint.h>

typedef unsigned v4u __attribute__((ext_vector_type(4)));
typedef unsigned v2u __attribute__((ext_vector_type(2)));
typedef float f4 __attribute__((ext_vector_type(4)));
typedef __bf16 bf8 __attribute__((ext_vector_type(8)));
typedef short s4 __attribute__((ext_vector_type(4)));
typedef __amdgpu_buffer_rsrc_t rsrc_t;
typedef unsigned long long u64;

#define STAMPS 0

constexpr int NWG = 256, NTHR = 704;
constexpr int SK = 8192, WIN = 2048, DH = 128, DM = 1024, NQ = 4096;
constexpr unsigned WS_EPOCH = 0;
constexpr unsigned WS_QP = 4096;
constexpr unsigned WS_Q1 = WS_QP + 2 * 4 * 4096 * 4;
constexpr unsigned WS_ML = WS_Q1 + 256 * 4096;
constexpr unsigned WS_OP = WS_ML + 2 * 16 * 16 * 128;
constexpr unsigned WS_P2 = WS_OP + 2 * 4 * 32 * 16 * 512;
constexpr unsigned WS_END = WS_P2 + 2 * 2 * 64 * 64 * 128;
constexpr unsigned WS_STAMP = WS_END;
#define AUXS 16
#define AUXL 16
constexpr int AUX_NT = 2, AUX_SC1 = 16;
constexpr unsigned SPIN_CAP = 1u << 14, LSPIN_CAP = 1u << 18;

struct Params {
  const void *xA, *kA, *vA, *wqA, *woA, *xB, *kB, *vB, *wqB, *woB;
  void *out, *ws;
};

__device__ __forceinline__ rsrc_t mk(const void* p, unsigned bytes) { return __builtin_amdgcn_make_buffer_rsrc((void*)p, 0, (int)bytes, 0x00020000); }
__device__ __forceinline__ v4u ldnt(rsrc_t r, int voff, int soff) { return __builtin_amdgcn_raw_buffer_load_b128(r, voff, soff, AUX_NT); }
__device__ __forceinline__ v4u ldc(rsrc_t r, int voff, int soff) { return __builtin_amdgcn_raw_buffer_load_b128(r, voff, soff, AUXL); }
__device__ __forceinline__ void stc(v4u v, rsrc_t r, int voff, int soff) { __builtin_amdgcn_raw_buffer_store_b128(v, r, voff, soff, AUXS); }
__device__ __forceinline__ f4 mfma32(v4u a, v4u b, f4 c) { return __builtin_amdgcn_mfma_f32_16x16x32_bf16(__builtin_bit_cast(bf8, a), __builtin_bit_cast(bf8, b), c, 0, 0, 0); }
__device__ __forceinline__ f4 mfma16(v2u a, v2u b, f4 c) { return __builtin_amdgcn_mfma_f32_16x16x16bf16_1k(__builtin_bit_cast(s4, a), __builtin_bit_cast(s4, b), c, 0, 0, 0); }
__device__ __forceinline__ unsigned fu(float x) { return __builtin_bit_cast(unsigned, x); }
__device__ __forceinline__ float uf(unsigned x) { return __builtin_bit_cast(float, x); }
__device__ __forceinline__ unsigned ror8u(unsigned x) { return (unsigned)__builtin_amdgcn_mov_dpp((int)x, 0x128, 0xf, 0xf, true); }
__device__ __forceinline__ float ror8(float x) { return uf(ror8u(fu(x))); }
__device__ __forceinline__ unsigned pk2(float lo, float hi) {
  typedef __bf16 bf2 __attribute__((ext_vector_type(2)));
  typedef float f2 __attribute__((ext_vector_type(2)));
  f2 v = {lo, hi};
  return __builtin_bit_cast(unsigned, __builtin_convertvector(v, bf2));
}
template <int T> __device__ __forceinline__ unsigned tr(const v4u& a, const v4u& b) {
  return __builtin_amdgcn_perm(b[T >> 1], a[T >> 1], (T & 1) ? 0x07060302u : 0x05040100u);
}
template <int T> __device__ __forceinline__ v4u tr8(const v4u* r) { return v4u{tr<T>(r[0], r[1]), tr<T>(r[2], r[3]), tr<T>(r[4], r[5]), tr<T>(r[6], r[7])}; }
template <int T> __device__ __forceinline__ v2u tr4(const v4u* r) { return v2u{tr<T>(r[0], r[1]), tr<T>(r[2], r[3])}; }
__device__ __forceinline__ float red32(float a, float b) { auto s = __builtin_amdgcn_permlane32_swap(fu(a), fu(b), false, false); return uf(s[0]) + uf(s[1]); }
__device__ __forceinline__ float red16(float a, float b) { auto s = __builtin_amdgcn_permlane16_swap(fu(a), fu(b), false, false); return uf(s[0]) + uf(s[1]); }
__device__ __forceinline__ float max_x8_16_32(float x) {
  x = fmaxf(x, ror8(x));
  auto s = __builtin_amdgcn_permlane16_swap(fu(x), fu(x), false, false); x = fmaxf(uf(s[0]), uf(s[1]));
  auto t = __builtin_amdgcn_permlane32_swap(fu(x), fu(x), false, false); return fmaxf(uf(t[0]), uf(t[1]));
}
__device__ __forceinline__ float sum_x8_16_32(float x) { x = x + ror8(x); x = red16(x, x); return red32(x, x); }
__device__ __forceinline__ v4u mark(f4 v, unsigned tf) {
  const v4u u = __builtin_bit_cast(v4u, v);
  return v4u{(u[0] & ~15u) | (tf & 15u), (u[1] & ~15u) | ((tf >> 4) & 15u), (u[2] & ~15u) | ((tf >> 8) & 15u), (u[3] & ~15u) | ((tf >> 12) & 15u)};
}
__device__ __forceinline__ unsigned mis(const v4u& g, unsigned tf) { return ((g[0] ^ tf) | (g[1] ^ (tf >> 4)) | (g[2] ^ (tf >> 8)) | (g[3] ^ (tf >> 12))) & 15u; }
__device__ __forceinline__ f4 val(const v4u& g) { return __builtin_bit_cast(f4, g & ~15u); }
__device__ __forceinline__ bool wave_bad(bool bad) { return __builtin_amdgcn_ballot_w64(bad) != 0ull; }
#define PIN() __builtin_amdgcn_sched_barrier(0)
#define VMW(n) asm volatile("s_waitcnt vmcnt(" #n ")" ::: "memory")
#define BAR() do { asm volatile("s_waitcnt lgkmcnt(0)" ::: "memory"); __builtin_amdgcn_s_barrier(); asm volatile("" ::: "memory"); } while (0)

__device__ __forceinline__ void tr64(const v4u* wr, v4u* wt) {
  wt[0] = tr8<0>(wr); wt[1] = tr8<1>(wr); wt[2] = tr8<2>(wr); wt[3] = tr8<3>(wr);
  wt[4] = tr8<4>(wr); wt[5] = tr8<5>(wr); wt[6] = tr8<6>(wr); wt[7] = tr8<7>(wr);
}
__device__ __forceinline__ void tile64_acc(const v4u* wt, v4u a, f4* d) {
#pragma unroll
  for (int t = 0; t < 8; ++t) d[t] = mfma32(a, wt[t], d[t]);
}
__device__ __forceinline__ float sel(bool c, float a, float b) { asm volatile("" : "+v"(a), "+v"(b)); return c ? a : b; }
__device__ __forceinline__ f4 tile64_fin(const f4* d, bool hi8) {
  f4 r;
#pragma unroll
  for (int u = 0; u < 4; ++u) r[u] = sel(hi8, d[4 + u][1], d[u][0]) + ror8(sel(hi8, d[u][1], d[4 + u][0]));
  return r;
}
__device__ __forceinline__ void repoll(v4u& g, unsigned tf, int lane, rsrc_t r, int voff, int soff) {
  const bool bad = ((__builtin_amdgcn_ballot_w64(mis(g, tf) != 0u) >> (lane & 56)) & 0xffull) != 0ull;
  const v4u n = ldc(r, bad ? voff : 0x7ffffff0, soff);
  g = v4u{bad ? n[0] : g[0], bad ? n[1] : g[1], bad ? n[2] : g[2], bad ? n[3] : g[3]};
}
template <int N, int SLEEP> __device__ __forceinline__ void gather2(v4u* g, unsigned tf, int lane, rsrc_t r, int voff, int soff, int stride) {
#pragma unroll
  for (int u = 0; u < N; ++u) g[u] = ldc(r, voff, soff + (u & 3) * 1024 + (u >> 2) * stride);
#pragma unroll 1
  for (unsigned spins = 0; spins < SPIN_CAP; ++spins) {
    unsigned m = 0;
#pragma unroll
    for (int u = 0; u < N; ++u) m |= mis(g[u], tf);
    if (!wave_bad(m != 0u)) break;
    if (SLEEP) __builtin_amdgcn_s_sleep(SLEEP);
    asm volatile("" ::: "memory");
#pragma unroll
    for (int u = 0; u < N; ++u) repoll(g[u], tf, lane, r, voff, soff + (u & 3) * 1024 + (u >> 2) * stride);
  }
}
__device__ __forceinline__ u64 launch_tag(const void* ws) {
  u64 e;
  asm volatile("s_load_dwordx2 %0, %1, 0x0\n\ts_waitcnt lgkmcnt(0)" : "=s"(e) : "s"(ws) : "memory");
  u64 d = (e + 0x9E3779B97F4A7C15ull) * 0xBF58476D1CE4E5B9ull;
  d ^= d >> 29;
  return e ^ (d | 0x1111111111111111ull);
}
__device__ __forceinline__ void pv32(const v4u* vr, v4u pf, f4* o) {
  o[0] = mfma32(tr8<0>(vr), pf, o[0]); o[1] = mfma32(tr8<1>(vr), pf, o[1]); o[2] = mfma32(tr8<2>(vr), pf, o[2]); o[3] = mfma32(tr8<3>(vr), pf, o[3]);
  o[4] = mfma32(tr8<4>(vr), pf, o[4]); o[5] = mfma32(tr8<5>(vr), pf, o[5]); o[6] = mfma32(tr8<6>(vr), pf, o[6]); o[7] = mfma32(tr8<7>(vr), pf, o[7]);
}
__device__ __forceinline__ void put_o(float* red, const f4* o) {
#pragma unroll
  for (int r = 0; r < 4; ++r) {
    *(f4*)(red + 8 * r) = f4{o[0][r], o[1][r], o[2][r], o[3][r]};
    *(f4*)(red + 8 * r + 4) = f4{o[4][r], o[5][r], o[6][r], o[7][r]};
  }
}
__device__ __forceinline__ void qk16(const v4u* kr, v4u q0, v4u q1, bool t0lane, float& z0, float& z1) {
  const f4 z = {0.f, 0.f, 0.f, 0.f};
  f4 da = mfma32(kr[1], q1, mfma32(kr[0], q0, z));
  f4 db = mfma32(kr[3], q1, mfma32(kr[2], q0, z));
  const float x0 = t0lane ? da[0] : db[0], x1 = t0lane ? da[1] : db[1], x2 = t0lane ? da[2] : db[2], x3 = t0lane ? da[3] : db[3];
  auto s = __builtin_amdgcn_permlane32_swap(fu(x0), fu(x1), false, false);
  auto t = __builtin_amdgcn_permlane32_swap(fu(x2), fu(x3), false, false);
  z0 = uf(s[0]) + ror8(uf(s[1]));
  z1 = uf(t[0]) + ror8(uf(t[1]));
}

constexpr int NSLOT = 7;
constexpr int RSTR = 132;
constexpr int L_RING = 0;
constexpr int L_RED = L_RING + 2 * NSLOT * 8192;
constexpr int L_Q = L_RED + 8 * 8 * RSTR * 4;
constexpr int QSTR = 272;
constexpr int L_ML = L_Q + 2 * 8 * QSTR;
constexpr int L_O = L_ML + 2 * 8 * 8 * 8;
constexpr int L_WT = L_O + 512;
constexpr int L_CTL = L_WT + 256;
constexpr int L_END = L_CTL + 128;
enum { C_DONE = 0, C_FULL = 8, C_QR = 10, C_Q = 11, C_ML = 12, C_OB = 13, C_OBD = 14, C_OA = 15, C_WT = 16, C_O = 17, C_P2 = 18 };
typedef __attribute__((address_space(3))) unsigned lds_u32;
#define CB() asm volatile("" ::: "memory")
__device__ __forceinline__ void arrive(unsigned char* lds, int c, int lane) {
  CB();
  if (lane == 0) __hip_atomic_fetch_add((lds_u32*)(lds + L_CTL) + c, 1u, __ATOMIC_RELAXED, __HIP_MEMORY_SCOPE_WORKGROUP);
  CB();
}
__device__ __forceinline__ void setw(unsigned char* lds, int c, unsigned v) {
  CB();
  __hip_atomic_store((lds_u32*)(lds + L_CTL) + c, v, __ATOMIC_RELAXED, __HIP_MEMORY_SCOPE_WORKGROUP);
  CB();
}
template <int SLEEP> __device__ __forceinline__ void await(unsigned char* lds, int c, unsigned n) {
  CB();
#pragma unroll 1
  for (unsigned spins = 0; spins < LSPIN_CAP; ++spins) {
    if ((unsigned)__builtin_amdgcn_readfirstlane(__hip_atomic_load((lds_u32*)(lds + L_CTL) + c, __ATOMIC_RELAXED, __HIP_MEMORY_SCOPE_WORKGROUP)) >= n) break;
    if (SLEEP) __builtin_amdgcn_s_sleep(SLEEP);
  }
  CB();
}

__device__ __forceinline__ void until(u64 t0, unsigned ticks) {
#pragma unroll 1
  while ((unsigned)(__builtin_amdgcn_s_memrealtime() - t0) < ticks) __builtin_amdgcn_s_sleep(2);
}

constexpr int NSTAMP = 16;
#define STAMP(i)
#define STAMP_OUT()

#define T1 580
#define T2 850
#define DBG 0
#define PRE2 1
#define FM 22
#define S1 10
#define S4 1
#define HOP1EARLY 0
#define WND 12
#define RELOAD() do { __builtin_amdgcn_s_sleep(1); CB(); } while (0)
typedef int v4i __attribute__((ext_vector_type(4)));
__device__ __forceinline__ v4i srd(const void* p, unsigned bytes) { const u64 a = (u64)p; return v4i{(int)(unsigned)a, (int)(unsigned)(a >> 32) & 0xffff, (int)bytes, 0x00020000}; }
__device__ __forceinline__ void dma(int m0, int voff, v4i r, int soff) {
  asm volatile("s_mov_b32 m0, %0\n\ts_nop 0\n\tbuffer_load_dwordx4 %1, %2, %3 offen nt lds" :: "s"(m0), "v"(voff), "s"(r), "s"(soff) : "memory");
}
template <int N> __device__ __forceinline__ void landed(int full_addr, int cnt) {
  asm volatile("s_waitcnt vmcnt(%2)\n\tds_write_b32 %0, %1" :: "v"(full_addr), "v"(cnt), "i"(N) : "memory");
}
template <int N, int LO> __device__ __forceinline__ void drain2(int full_addr, int total, int& pub) {
  if constexpr (N >= LO) {
    pub = max(total - N, pub);
    landed<N>(full_addr, pub);
    drain2<N - 2, LO>(full_addr, total, pub);
  }
}
template <int V> struct IC { static constexpr int value = V; };

__global__ void __launch_bounds__(NTHR, 3) fused_decode(Params p) {
  __shared__ __attribute__((aligned(16))) unsigned char lds[L_END];
  u64 th = 0;
#define HMARK(i) do { if (HOLDL == (i)) { PIN(); asm volatile("s_memrealtime %0\n s_waitcnt lgkmcnt(0)" : "=s"(th) :: "memory"); PIN(); } } while (0)
  HMARK(0);
  const int tid = threadIdx.x, lane = tid & 63, wv = __builtin_amdgcn_readfirstlane(tid >> 6);
  const int j = blockIdx.x;
  const int G = lane >> 4, c16 = lane & 15;
  const bool hi8 = (lane & 8) != 0;
  const int pair = j >> 4, b = pair >> 2, hkv = pair & 3, s = j & 15;
  const int br = j >> 7, cb = (j & 127) >> 3, ri = (j & 127) >> 1, ch = j & 1;
  const unsigned KVB = 16u * SK * DH * 2, WB = DM * NQ * 2;

  if (__builtin_expect(wv < 2, 1)) {
    __builtin_amdgcn_s_setprio(3);
    const int l = wv;
    const int rot = j * 5 + (j >> 3);
    const int lin = lane * 16;
    const int vk0 = G * 256 + ((c16 ^ (2 * G)) * 16), vk1 = G * 256 + ((c16 ^ (8 + 2 * G)) * 16);
    const int voq = (8 * (lane >> 3)) * (NQ * 2) + (lane & 7) * 16, voo = (8 * (lane >> 3)) * (DM * 2) + (lane & 7) * 16;
    const int lds0 = (int)(unsigned)(size_t)(__attribute__((address_space(3))) unsigned char*)lds;
    const int ring = lds0 + L_RING + l * (NSLOT * 8192), full_addr = lds0 + L_CTL + (C_FULL + l) * 4, done_addr = lds0 + L_CTL + (C_DONE + 4 * l) * 4;
    int f = 0, slot = 0, pub = 0, freed = NSLOT;
    auto fill = [&](auto wc, v4i r, int v0, int v1, int base, int sc, int sh) {
      constexpr int W = decltype(wc)::value;
      if (f >= freed) {
        pub = 8 * f;
        landed<0>(full_addr, pub);
#pragma unroll 1
        for (unsigned spins = 0; spins < LSPIN_CAP; ++spins) {
          v4i d;
          asm volatile("ds_read_b128 %0, %1\n\ts_waitcnt lgkmcnt(0)" : "=v"(d) : "v"(done_addr) : "memory");
          freed = NSLOT + __builtin_amdgcn_readfirstlane(min(min(d[0], d[1]), min(d[2], d[3])));
          if (f < freed) break;
          __builtin_amdgcn_s_sleep(1);
        }
      } else if (f + 2 >= freed) {
        v4i d;
        asm volatile("ds_read_b128 %0, %1\n\ts_waitcnt lgkmcnt(0)" : "=v"(d) : "v"(done_addr) : "memory");
        freed = NSLOT + __builtin_amdgcn_readfirstlane(min(min(d[0], d[1]), min(d[2], d[3])));
      }
      const int dst = ring + slot * 8192;
#pragma unroll
      for (int c = 0; c < 4; ++c) {
        if constexpr (W > 0) { pub = max(8 * f + 2 * c - (W - 2), pub); landed<W - 2>(full_addr, pub); }
        dma(dst + c * 2048, v0, r, base + c * sc);
        dma(dst + c * 2048 + 1024, v1, r, base + c * sc + sh);
      }
      ++f; slot = slot == NSLOT - 1 ? 0 : slot + 1;
    };
    const int baseA = (pair * SK + s * 512 + l * 32) * 256, baseB = (pair * SK + (SK - WIN) + s * 128 + l * 32) * 256;
    {
      const v4i r = srd(br ? p.wqB : p.wqA, WB);
      const int baseQ = (128 * (j & 7) + 64 * l) * (NQ * 2) + cb * 512;
      fill(IC<0>{}, r, voq, voq, baseQ, 128, NQ * 2);
      HMARK(1);
      __builtin_amdgcn_s_barrier();
      HMARK(2);
#pragma unroll 1
      for (int t = 1; t < 4; ++t) fill(IC<WND>{}, r, voq, voq, baseQ + 2 * t * (NQ * 2), 128, NQ * 2);
    }
    STAMP(1);
    HMARK(3);
    {
      const v4i r = srd(p.kA, KVB);
#pragma unroll 1
      for (int t = 0; t < 8; ++t) fill(IC<WND>{}, r, vk0, vk1, baseA + (((t + rot) & 7) << 14), 2048, 1024);
    }
    {
      const v4i r = srd(p.kB, KVB);
#pragma unroll 1
      for (int t = 0; t < 2; ++t) fill(IC<WND>{}, r, vk0, vk1, baseB + (t << 14), 2048, 1024);
    }
    STAMP(2);
    {
      const v4i r = srd(p.vB, KVB);
#pragma unroll 1
      for (int t = 0; t < 2; ++t) fill(IC<WND>{}, r, lin, lin, baseB + (t << 14), 2048, 1024);
    }
    {
      const v4i r = srd(p.vA, KVB);
#pragma unroll 1
      for (int t = 0; t < 8; ++t) fill(IC<WND>{}, r, lin, lin, baseA + (((t + rot) & 7) << 14), 2048, 1024);
    }
    STAMP(3);
    HMARK(4);
    {
      const v4i r = srd(br ? p.woB : p.woA, WB);
      const int baseO = (64 * ri) * (DM * 2) + (512 * ch + 256 * l) * 2;
#pragma unroll 1
      for (int t = 0; t < 4; ++t) fill(IC<WND>{}, r, voo, voo, baseO + 2 * t * (DM * 2), 128, DM * 2);
    }
    STAMP(4);
    drain2<WND - 2, 0>(full_addr, 8 * 28, pub);
    HMARK(5);
    until(th, 5000);
    STAMP(5);
    STAMP_OUT();
    return;
  }

  const rsrc_t rws = mk(p.ws, WS_END);
  const f4 z = {0.f, 0.f, 0.f, 0.f};
  if (wv == 2) {
    __builtin_amdgcn_s_setprio(2);
    const u64 t0 = __builtin_amdgcn_s_memrealtime();
    if (lane < 32) *(unsigned*)(lds + L_CTL + lane * 4) = 0u;
    asm volatile("s_waitcnt lgkmcnt(0)" ::: "memory");
    __builtin_amdgcn_s_barrier();
    const u64 tag = launch_tag(p.ws);
    const unsigned tl = (unsigned)(tag >> (16 * (lane & 3)));
    await<2>(lds, C_QR, 8);
    {
      const int un = 2 * (lane & 3) + (hi8 ? 1 : 0);
      const unsigned tu = (unsigned)(tag >> (16 * (un & 3)));
#pragma unroll
      for (int u = 0; u < 4; ++u)
        stc(mark(*(const f4*)(lds + L_RED + u * 1024 + lane * 16) + *(const f4*)(lds + L_RED + (u + 4) * 1024 + lane * 16), tu), rws,
            ((lane >> 2) & 1) * 4096 + G * 128 + un * 16, WS_Q1 + ((j & ~7) + 2 * u) * 4096 + (j & 7) * 512);
    }
    STAMP(3);
    until(t0, T1);
    {
      v4u g[4];
      gather2<4, S1>(g, tl, lane, rws, lane * 16, WS_Q1 + j * 4096, 0);
      const f4 r = (val(g[0]) + val(g[1])) + (val(g[2]) + val(g[3]));
      const unsigned r01 = pk2(red32(r[0], r[0]), red32(r[1], r[1])), r23 = pk2(red32(r[2], r[2]), red32(r[3], r[3]));
      if (lane < 32)
        stc(mark(f4{uf(r01 << 16), uf(r01 & 0xffff0000u), uf(r23 << 16), uf(r23 & 0xffff0000u)}, tl), rws,
            (lane >> 3) * (NQ * 4) + (lane & 7) * 16, WS_QP + (br * 4 * NQ + 32 * (j & 127)) * 4);
    }
    STAMP(4);
    until(t0, T2);
    {
      v4u g[8];
      gather2<8, S1>(g, tl, lane, rws, lane * 16, WS_QP + (b * NQ + hkv * 1024) * 4, 4 * NQ * 4);
#pragma unroll
      for (int u = 0; u < 8; ++u)
        *(v2u*)(lds + L_Q + (2 * u + (lane >> 5)) * QSTR + (lane & 31) * 8) =
            v2u{__builtin_amdgcn_perm(g[u][1], g[u][0], 0x07060302u), __builtin_amdgcn_perm(g[u][3], g[u][2], 0x07060302u)};
    }
    setw(lds, C_Q, 1u);
    STAMP(5);
    await<2>(lds, C_ML, 8);
    {
      f4 mv[4];
#pragma unroll
      for (int u = 0; u < 4; ++u) mv[u] = *(const f4*)(lds + L_ML + c16 * 64 + u * 16);
      float M = -INFINITY, L = 0.f;
#pragma unroll
      for (int u = 0; u < 4; ++u) M = fmaxf(M, fmaxf(mv[u][0], mv[u][2]));
#pragma unroll
      for (int u = 0; u < 4; ++u) { L = fmaf(__builtin_amdgcn_exp2f(mv[u][0] - M), mv[u][1], L); L = fmaf(__builtin_amdgcn_exp2f(mv[u][2] - M), mv[u][3], L); }
      if (lane < 16) stc(v4u{fu(M), (unsigned)tag, fu(L), (unsigned)(tag >> 32)}, rws, (hi8 ? 256 * 128 : 0) + (lane & 7) * 16, WS_ML + j * 128);
    }
    STAMP(6);
    STAMP(7);
    await<4>(lds, C_FULL, 8 * FM);
    {
      const int mo = (G * 4 * 16 + c16) * 128, mg = WS_ML + ((br * 16 + (ri >> 4)) * 16) * 128 + ((ri >> 1) & 7) * 16;
      v4u ml = ldc(rws, mo, mg);
#pragma unroll 1
      for (unsigned spins = 0; spins < SPIN_CAP; ++spins) {
        const bool bad = ml[1] != (unsigned)tag || ml[3] != (unsigned)(tag >> 32);
        if (!wave_bad(bad)) break;
        __builtin_amdgcn_s_sleep(S1); CB();
        const v4u n = ldc(rws, bad ? mo : 0x7ffffff0, mg);
        ml = v4u{bad ? n[0] : ml[0], bad ? n[1] : ml[1], bad ? n[2] : ml[2], bad ? n[3] : ml[3]};
      }
      const float m = uf(ml[0]);
      float M = m;
      M = fmaxf(M, __shfl_xor(M, 1)); M = fmaxf(M, __shfl_xor(M, 2)); M = fmaxf(M, __shfl_xor(M, 4)); M = fmaxf(M, ror8(M));
      const float w0 = __builtin_amdgcn_exp2f(m - M);
      float L = w0 * uf(ml[2]);
      L += __shfl_xor(L, 1); L += __shfl_xor(L, 2); L += __shfl_xor(L, 4); L += ror8(L);
      *(float*)(lds + L_WT + (G * 8 + (lane & 7)) * 8 + (hi8 ? 4 : 0)) = w0 / L;
      setw(lds, C_WT, 1u);
    }
    STAMP(8);
    await<1>(lds, C_P2, 8);
    STAMP(9);
    {
      v4u g[8];
      gather2<8, 0>(g, tl, lane, rws, lane * 16, WS_P2 + ((br * 2 + ch) * 64 + ri) * (64 * 128), 4096);
      f4 v = val(g[0]);
#pragma unroll
      for (int u = 1; u < 8; ++u) v += val(g[u]);
      const float y0 = red32(v[0], v[2]), y1 = red32(v[1], v[3]);
      float y = red16(y0, y1);
      y = y + ror8(y);
      if (!hi8) {
        const int pc = lane & 7;
        ((__bf16*)p.out)[(pc >> 1) * 2048 + br * 1024 + 512 * ch + 8 * ri + 4 * (pc & 1) + 2 * (lane >> 5) + (G & 1)] = (__bf16)y;
      }
      if (j == 0 && lane == 0) __builtin_amdgcn_raw_buffer_store_b64(v2u{(unsigned)tag, (unsigned)(tag >> 32)}, rws, 0, WS_EPOCH, AUX_SC1);
    }
    STAMP(10);
    STAMP_OUT();
    return;
  }

  const int w = wv - 3;
  const v4u xa = __builtin_amdgcn_raw_buffer_load_b128(mk(br ? p.xB : p.xA, 4 * DM * 2), ((c16 >> 2) * DM + 8 * (2 * G + (c16 & 1))) * 2,
                                                       (128 * (j & 7) + 64 * (w >> 2)) * 2, 0);
  const unsigned char* mine = lds + L_RING + (w >> 2) * (NSLOT * 8192) + (w & 3) * 2048;
  const int kr0 = (lane & 7) * 256 + (((hi8 ? 4 : 0) + G) ^ (2 * (lane & 7))) * 16;
  const int vr0 = (4 * (G & 1) + (G >> 1)) * 256 + c16 * 16;
  __builtin_amdgcn_s_barrier();
  const u64 tag = launch_tag(p.ws);
#define RD(f, o) (*(const v4u*)(mine + ((f) % NSLOT) * 8192 + (o)))
#define TAKE(f, N, dst, T, O0, O1, SL) do { await<SL>(lds, C_FULL + (w >> 2), 8 * ((f) + (N) - 1) + 2 * (w & 3) + 2); \
    _Pragma("unroll") for (int u_ = 0; u_ < (N); ++u_) { const int t_ = (T) + u_; (void)t_; dst[2 * t_] = RD((f) + u_, O0); dst[2 * t_ + 1] = RD((f) + u_, O1); } \
    asm volatile("s_waitcnt lgkmcnt(0)" ::: "memory"); setw(lds, C_DONE + w, (f) + (N)); \
    _Pragma("unroll") for (int u_ = 0; u_ < 2 * (N); ++u_) asm volatile("" : "+v"(dst[2 * (T) + u_])); } while (0)
#define TAKEK(f, N, dst, T) TAKE(f, N, dst, T, kr0 ^ ((t_ & 1) * 64), kr0 ^ ((t_ & 1) * 64) ^ 128, 2)
#define TAKEV(f, N, dst, T, SL) TAKE(f, N, dst, T, vr0, vr0 + 512, SL)
#define TAKEW(f, N, dst, T, SL) TAKE(f, N, dst, T, lane * 16, lane * 16 + 1024, SL)
  v4u kB[4], vB[4], kA[16], vt[8], wt[8];
  {
    f4 d[8] = {z, z, z, z, z, z, z, z};
    TAKEW(0, 4, vt, 0, 1);
    tr64(vt, wt); tile64_acc(wt, xa, d);
    *(f4*)(lds + L_RED + w * 1024 + lane * 16) = tile64_fin(d, hi8);
    arrive(lds, C_QR, lane);
  }
  STAMP(1);
  TAKEK(4, 2, kA, 0); TAKEK(6, 2, kA, 2); TAKEK(8, 2, kA, 4); TAKEK(10, 2, kA, 6); TAKEK(12, 2, kB, 0);
  TAKEV(14, 2, vB, 0, 2);
  TAKEV(16, 2, vt, 0, 2);
  STAMP(2);
  await<2>(lds, C_Q, 1);
  STAMP(3);
  constexpr float C2 = 0.088388347648318440f * 1.4426950408889634f;
  unsigned pA[4], pB;
  float* red = (float*)(lds + L_RED) + (w * 8 + (lane & 7)) * RSTR + 32 * G;
  {
    const unsigned char* qp = lds + L_Q + (lane & 7) * QSTR + (hi8 ? 64 : 0) + G * 16;
    const bool t0lane = (G >> 1) == (c16 >> 3);
    float za[8], zb[2];
    const v4u qa0 = *(const v4u*)qp, qa1 = *(const v4u*)(qp + 128), qb0 = *(const v4u*)(qp + 8 * QSTR), qb1 = *(const v4u*)(qp + 8 * QSTR + 128);
#pragma unroll
    for (int u = 0; u < 4; ++u) qk16(kA + 4 * u, qa0, qa1, t0lane, za[2 * u], za[2 * u + 1]);
    qk16(kB, qb0, qb1, t0lane, zb[0], zb[1]);
    float mA = fmaxf(fmaxf(fmaxf(za[0], za[1]), fmaxf(za[2], za[3])), fmaxf(fmaxf(za[4], za[5]), fmaxf(za[6], za[7])));
    mA = max_x8_16_32(mA) * C2;
    const float mB = max_x8_16_32(fmaxf(zb[0], zb[1])) * C2;
    float lA = 0.f;
#pragma unroll
    for (int u = 0; u < 8; ++u) { za[u] = __builtin_amdgcn_exp2f(fmaf(za[u], C2, -mA)); lA += za[u]; }
    zb[0] = __builtin_amdgcn_exp2f(fmaf(zb[0], C2, -mB)); zb[1] = __builtin_amdgcn_exp2f(fmaf(zb[1], C2, -mB));
    lA = sum_x8_16_32(lA);
    const float lB = sum_x8_16_32(zb[0] + zb[1]);
    if (lane < 16) *(float2*)(lds + L_ML + (lane * 8 + w) * 8) = hi8 ? make_float2(mB, lB) : make_float2(mA, lA);
    arrive(lds, C_ML, lane);
    await<1>(lds, C_ML, 8);
    float MA = -INFINITY, MB = -INFINITY;
#pragma unroll
    for (int u = 0; u < 4; ++u) {
      const f4 ma = *(const f4*)(lds + L_ML + (lane & 7) * 64 + u * 16), mb = *(const f4*)(lds + L_ML + (8 + (lane & 7)) * 64 + u * 16);
      MA = fmaxf(MA, fmaxf(ma[0], ma[2])); MB = fmaxf(MB, fmaxf(mb[0], mb[2]));
    }
    const float fA = __builtin_amdgcn_exp2f(mA - MA), fB = __builtin_amdgcn_exp2f(mB - MB);
#pragma unroll
    for (int u = 0; u < 4; ++u) pA[u] = pk2(za[2 * u] * fA, za[2 * u + 1] * fA);
    pB = pk2(zb[0] * fB, zb[1] * fB);
  }
  STAMP(4);
  const unsigned tl = (unsigned)(tag >> (16 * (lane & 3)));
  const float* rd = (const float*)(lds + L_RED) + (2 * (w & 3) + (lane >> 5)) * RSTR + (lane & 31) * 4;
  const int opv = (lane >> 5) * (16 * 512) + (lane & 31) * 16, ops = WS_OP + (((w >> 2) * 4 + b) * 32 + hkv * 8 + 2 * (w & 3)) * (16 * 512) + s * 512;
  {
    const v2u pb = {pB, ror8u(pB)};
    f4 o[8];
    o[0] = mfma16(tr4<0>(vB), pb, z); o[1] = mfma16(tr4<1>(vB), pb, z); o[2] = mfma16(tr4<2>(vB), pb, z); o[3] = mfma16(tr4<3>(vB), pb, z);
    o[4] = mfma16(tr4<4>(vB), pb, z); o[5] = mfma16(tr4<5>(vB), pb, z); o[6] = mfma16(tr4<6>(vB), pb, z); o[7] = mfma16(tr4<7>(vB), pb, z);
    if (!hi8) put_o(red, o);
    arrive(lds, C_OB, lane);
  }
  if (w >= 4) {
    await<1>(lds, C_OB, 8);
    f4 r = *(const f4*)rd;
#pragma unroll
    for (int u = 1; u < 8; ++u) r += *(const f4*)(rd + u * 8 * RSTR);
    stc(mark(r, tl), rws, opv, ops);
    arrive(lds, C_OBD, lane);
  }
  TAKEV(18, 2, vt, 2, 1);
  f4 o[8] = {z, z, z, z, z, z, z, z};
  pv32(vt, v4u{pA[0], ror8u(pA[0]), pA[1], ror8u(pA[1])}, o);
#pragma unroll
  for (int u = 0; u < 8; ++u) asm volatile("" : "+v"(o[u]));
  STAMP(5);
  TAKEV(20, 3, vt, 0, 1);
  await<1>(lds, C_OBD, 4);
  TAKEV(23, 1, vt, 3, 0);
  STAMP(6);
  pv32(vt, v4u{pA[2], ror8u(pA[2]), pA[3], ror8u(pA[3])}, o);
  if (!hi8) put_o(red, o);
  arrive(lds, C_OA, lane);
  if (w < 4) {
    await<0>(lds, C_OA, 8);
    f4 r = *(const f4*)rd;
#pragma unroll
    for (int u = 1; u < 8; ++u) r += *(const f4*)(rd + u * 8 * RSTR);
    stc(mark(r, tl), rws, opv, ops);
  }
  STAMP(7);
  TAKEW(24, 2, vt, 0, 1);
  TAKEW(26, 2, vt, 2, 0);
  {
    const int og = WS_OP + ((br * 4 + (w >> 1)) * 32 + (ri >> 1)) * (16 * 512) + (64 * (ri & 1) + 32 * (w & 1)) * 4;
    const int ov = (lane >> 3) * 512 + (lane & 7) * 16, o1 = 8 * 512;
    v4u g0 = ldc(rws, ov, og), g1 = ldc(rws, ov, og + o1);
    PIN();
    STAMP(8);
    tr64(vt, wt);
#pragma unroll
    for (int u = 0; u < 8; ++u) asm volatile("" : "+v"(wt[u]));
    await<1>(lds, C_WT, 1);
    const float2 wgt = *(const float2*)(lds + L_WT + ((w >> 1) * 8 + (lane >> 3)) * 8);
    PIN();
#pragma unroll 1
    for (unsigned spins = 0; spins < SPIN_CAP; ++spins) {
      if (!wave_bad((mis(g0, tl) | mis(g1, tl)) != 0u)) break;
      __builtin_amdgcn_s_sleep(S4); CB();
      repoll(g0, tl, lane, rws, ov, og);
      repoll(g1, tl, lane, rws, ov, og + o1);
    }
    STAMP(9);
    const f4 v = val(g0) * wgt.x + val(g1) * wgt.y;
    const float y0 = red32(v[0], v[2]), y1 = red32(v[1], v[3]);
    float y = red16(y0, y1);
    y = y + ror8(y);
    *(__bf16*)(lds + L_O + ((w >> 1) * 64 + 32 * (w & 1) + 4 * (lane & 7) + 2 * (lane >> 5) + (G & 1)) * 2) = (__bf16)y;
  }
  arrive(lds, C_O, lane);
  await<0>(lds, C_O, 8);
  {
    const v4u a = *(const v4u*)(lds + L_O + ((c16 >> 2) * 64 + 8 * (2 * G + (c16 & 1))) * 2);
    f4 d[8] = {z, z, z, z, z, z, z, z};
    tile64_acc(wt, a, d);
    stc(mark(tile64_fin(d, hi8), (unsigned)(tag >> (16 * ((2 * G + (hi8 ? 1 : 0)) & 3)))), rws, (lane & 7) * (64 * 128) + G * 32 + (hi8 ? 16 : 0),
        WS_P2 + (((br * 2 + ch) * 64 + 8 * w) * 64 + ri) * 128);
  }
  arrive(lds, C_P2, lane);
  STAMP(10);
  STAMP_OUT();
}

extern "C" void kernel_launch(void* const* d_in, const int* in_sizes, int n_in, void* d_out, int out_size, void* d_ws, size_t ws_size,
                              hipStream_t stream) {
  Params p;
  p.xA = d_in[0]; p.kA = d_in[1]; p.vA = d_in[2]; p.wqA = d_in[3]; p.woA = d_in[4];
  p.xB = d_in[5]; p.kB = d_in[6]; p.vB = d_in[7]; p.wqB = d_in[8]; p.woB = d_in[9];
  p.out = d_out; p.ws = d_ws;
  fused_decode<<<NWG, NTHR, 0, stream>>>(p);
}
```

```cpp
#include <hip/hip_runtime.h>
#include <hip/hip_bf16.h>
#include <stdint.h>

typedef unsigned v4u __attribute__((ext_vector_type(4)));
typedef unsigned v2u __attribute__((ext_vector_type(2)));
typedef float f4 __attribute__((ext_vector_type(4)));
typedef __bf16 bf8 __attribute__((ext_vector_type(8)));
typedef short s4 __attribute__((ext_vector_type(4)));
typedef __amdgpu_buffer_rsrc_t rsrc_t;
typedef unsigned long long u64;

#define STAMPS 0

constexpr int NWG = 256, NTHR = 704;
constexpr int SK = 8192, WIN = 2048, DH = 128, DM = 1024, NQ = 4096;
constexpr unsigned WS_EPOCH = 0;
constexpr unsigned WS_QP = 4096;
constexpr unsigned WS_Q1 = WS_QP + 2 * 4 * 4096 * 4;
constexpr unsigned WS_ML = WS_Q1 + 256 * 4096;
constexpr unsigned WS_OP = WS_ML + 2 * 16 * 16 * 128;
constexpr unsigned WS_P2 = WS_OP + 2 * 4 * 32 * 16 * 512;
constexpr unsigned WS_END = WS_P2 + 2 * 2 * 64 * 64 * 128;
constexpr unsigned WS_STAMP = WS_END;
#define AUXS 16
#define AUXL 16
constexpr int AUX_NT = 2, AUX_SC1 = 16;
constexpr unsigned SPIN_CAP = 1u << 14, LSPIN_CAP = 1u << 18;

struct Params {
  const void *xA, *kA, *vA, *wqA, *woA, *xB, *kB, *vB, *wqB, *woB;
  void *out, *ws;
};

__device__ __forceinline__ rsrc_t mk(const void* p, unsigned bytes) { return __builtin_amdgcn_make_buffer_rsrc((void*)p, 0, (int)bytes, 0x00020000); }
__device__ __forceinline__ v4u ldnt(rsrc_t r, int voff, int soff) { return __builtin_amdgcn_raw_buffer_load_b128(r, voff, soff, AUX_NT); }
__device__ __forceinline__ v4u ldc(rsrc_t r, int voff, int soff) { return __builtin_amdgcn_raw_buffer_load_b128(r, voff, soff, AUXL); }
__device__ __forceinline__ void stc(v4u v, rsrc_t r, int voff, int soff) { __builtin_amdgcn_raw_buffer_store_b128(v, r, voff, soff, AUXS); }
__device__ __forceinline__ f4 mfma32(v4u a, v4u b, f4 c) { return __builtin_amdgcn_mfma_f32_16x16x32_bf16(__builtin_bit_cast(bf8, a), __builtin_bit_cast(bf8, b), c, 0, 0, 0); }
__device__ __forceinline__ f4 mfma16(v2u a, v2u b, f4 c) { return __builtin_amdgcn_mfma_f32_16x16x16bf16_1k(__builtin_bit_cast(s4, a), __builtin_bit_cast(s4, b), c, 0, 0, 0); }
__device__ __forceinline__ unsigned fu(float x) { return __builtin_bit_cast(unsigned, x); }
__device__ __forceinline__ float uf(unsigned x) { return __builtin_bit_cast(float, x); }
__device__ __forceinline__ unsigned ror8u(unsigned x) { return (unsigned)__builtin_amdgcn_mov_dpp((int)x, 0x128, 0xf, 0xf, true); }
__device__ __forceinline__ float ror8(float x) { return uf(ror8u(fu(x))); }
__device__ __forceinline__ unsigned pk2(float lo, float hi) {
  typedef __bf16 bf2 __attribute__((ext_vector_type(2)));
  typedef float f2 __attribute__((ext_vector_type(2)));
  f2 v = {lo, hi};
  return __builtin_bit_cast(unsigned, __builtin_convertvector(v, bf2));
}
template <int T> __device__ __forceinline__ unsigned tr(const v4u& a, const v4u& b) {
  return __builtin_amdgcn_perm(b[T >> 1], a[T >> 1], (T & 1) ? 0x07060302u : 0x05040100u);
}
template <int T> __device__ __forceinline__ v4u tr8(const v4u* r) { return v4u{tr<T>(r[0], r[1]), tr<T>(r[2], r[3]), tr<T>(r[4], r[5]), tr<T>(r[6], r[7])}; }
template <int T> __device__ __forceinline__ v2u tr4(const v4u* r) { return v2u{tr<T>(r[0], r[1]), tr<T>(r[2], r[3])}; }
__device__ __forceinline__ float red32(float a, float b) { auto s = __builtin_amdgcn_permlane32_swap(fu(a), fu(b), false, false); return uf(s[0]) + uf(s[1]); }
__device__ __forceinline__ float red16(float a, float b) { auto s = __builtin_amdgcn_permlane16_swap(fu(a), fu(b), false, false); return uf(s[0]) + uf(s[1]); }
__device__ __forceinline__ float max_x8_16_32(float x) {
  x = fmaxf(x, ror8(x));
  auto s = __builtin_amdgcn_permlane16_swap(fu(x), fu(x), false, false); x = fmaxf(uf(s[0]), uf(s[1]));
  auto t = __builtin_amdgcn_permlane32_swap(fu(x), fu(x), false, false); return fmaxf(uf(t[0]), uf(t[1]));
}
__device__ __forceinline__ float sum_x8_16_32(float x) { x = x + ror8(x); x = red16(x, x); return red32(x, x); }
__device__ __forceinline__ v4u mark(f4 v, unsigned tf) {
  const v4u u = __builtin_bit_cast(v4u, v);
  return v4u{(u[0] & ~15u) | (tf & 15u), (u[1] & ~15u) | ((tf >> 4) & 15u), (u[2] & ~15u) | ((tf >> 8) & 15u), (u[3] & ~15u) | ((tf >> 12) & 15u)};
}
__device__ __forceinline__ unsigned mis(const v4u& g, unsigned tf) { return ((g[0] ^ tf) | (g[1] ^ (tf >> 4)) | (g[2] ^ (tf >> 8)) | (g[3] ^ (tf >> 12))) & 15u; }
__device__ __forceinline__ f4 val(const v4u& g) { return __builtin_bit_cast(f4, g & ~15u); }
__device__ __forceinline__ bool wave_bad(bool bad) { return __builtin_amdgcn_ballot_w64(bad) != 0ull; }
#define PIN() __builtin_amdgcn_sched_barrier(0)
#define VMW(n) asm volatile("s_waitcnt vmcnt(" #n ")" ::: "memory")
#define BAR() do { asm volatile("s_waitcnt lgkmcnt(0)" ::: "memory"); __builtin_amdgcn_s_barrier(); asm volatile("" ::: "memory"); } while (0)

__device__ __forceinline__ void tr64(const v4u* wr, v4u* wt) {
  wt[0] = tr8<0>(wr); wt[1] = tr8<1>(wr); wt[2] = tr8<2>(wr); wt[3] = tr8<3>(wr);
  wt[4] = tr8<4>(wr); wt[5] = tr8<5>(wr); wt[6] = tr8<6>(wr); wt[7] = tr8<7>(wr);
}
__device__ __forceinline__ void tile64_acc(const v4u* wt, v4u a, f4* d) {
#pragma unroll
  for (int t = 0; t < 8; ++t) d[t] = mfma32(a, wt[t], d[t]);
}
__device__ __forceinline__ float sel(bool c, float a, float b) { asm volatile("" : "+v"(a), "+v"(b)); return c ? a : b; }
__device__ __forceinline__ f4 tile64_fin(const f4* d, bool hi8) {
  f4 r;
#pragma unroll
  for (int u = 0; u < 4; ++u) r[u] = sel(hi8, d[4 + u][1], d[u][0]) + ror8(sel(hi8, d[u][1], d[4 + u][0]));
  return r;
}
__device__ __forceinline__ void repoll(v4u& g, unsigned tf, int lane, rsrc_t r, int voff, int soff) {
  const bool bad = ((__builtin_amdgcn_ballot_w64(mis(g, tf) != 0u) >> (lane & 56)) & 0xffull) != 0ull;
  const v4u n = ldc(r, bad ? voff : 0x7ffffff0, soff);
  g = v4u{bad ? n[0] : g[0], bad ? n[1] : g[1], bad ? n[2] : g[2], bad ? n[3] : g[3]};
}
template <int N, int SLEEP> __device__ __forceinline__ void gather2(v4u* g, unsigned tf, int lane, rsrc_t r, int voff, int soff, int stride) {
#pragma unroll
  for (int u = 0; u < N; ++u) g[u] = ldc(r, voff, soff + (u & 3) * 1024 + (u >> 2) * stride);
#pragma unroll 1
  for (unsigned spins = 0; spins < SPIN_CAP; ++spins) {
    unsigned m = 0;
#pragma unroll
    for (int u = 0; u < N; ++u) m |= mis(g[u], tf);
    if (!wave_bad(m != 0u)) break;
    if (SLEEP) __builtin_amdgcn_s_sleep(SLEEP);
    asm volatile("" ::: "memory");
#pragma unroll
    for (int u = 0; u < N; ++u) repoll(g[u], tf, lane, r, voff, soff + (u & 3) * 1024 + (u >> 2) * stride);
  }
}
__device__ __forceinline__ u64 launch_tag(const void* ws) {
  u64 e;
  asm volatile("s_load_dwordx2 %0, %1, 0x0\n\ts_waitcnt lgkmcnt(0)" : "=s"(e) : "s"(ws) : "memory");
  u64 d = (e + 0x9E3779B97F4A7C15ull) * 0xBF58476D1CE4E5B9ull;
  d ^= d >> 29;
  return e ^ (d | 0x1111111111111111ull);
}
__device__ __forceinline__ void pv32(const v4u* vr, v4u pf, f4* o) {
  o[0] = mfma32(tr8<0>(vr), pf, o[0]); o[1] = mfma32(tr8<1>(vr), pf, o[1]); o[2] = mfma32(tr8<2>(vr), pf, o[2]); o[3] = mfma32(tr8<3>(vr), pf, o[3]);
  o[4] = mfma32(tr8<4>(vr), pf, o[4]); o[5] = mfma32(tr8<5>(vr), pf, o[5]); o[6] = mfma32(tr8<6>(vr), pf, o[6]); o[7] = mfma32(tr8<7>(vr), pf, o[7]);
}
__device__ __forceinline__ void put_o(float* red, const f4* o) {
#pragma unroll
  for (int r = 0; r < 4; ++r) {
    *(f4*)(red + 8 * r) = f4{o[0][r], o[1][r], o[2][r], o[3][r]};
    *(f4*)(red + 8 * r + 4) = f4{o[4][r], o[5][r], o[6][r], o[7][r]};
  }
}
__device__ __forceinline__ void qk16(const v4u* kr, v4u q0, v4u q1, bool t0lane, float& z0, float& z1) {
  const f4 z = {0.f, 0.f, 0.f, 0.f};
  f4 da = mfma32(kr[1], q1, mfma32(kr[0], q0, z));
  f4 db = mfma32(kr[3], q1, mfma32(kr[2], q0, z));
  const float x0 = t0lane ? da[0] : db[0], x1 = t0lane ? da[1] : db[1], x2 = t0lane ? da[2] : db[2], x3 = t0lane ? da[3] : db[3];
  auto s = __builtin_amdgcn_permlane32_swap(fu(x0), fu(x1), false, false);
  auto t = __builtin_amdgcn_permlane32_swap(fu(x2), fu(x3), false, false);
  z0 = uf(s[0]) + ror8(uf(s[1]));
  z1 = uf(t[0]) + ror8(uf(t[1]));
}

constexpr int NSLOT = 7;
constexpr int RSTR = 132;
constexpr int L_RING = 0;
constexpr int L_RED = L_RING + 2 * NSLOT * 8192;
constexpr int L_Q = L_RED + 8 * 8 * RSTR * 4;
constexpr int QSTR = 272;
constexpr int L_ML = L_Q + 2 * 8 * QSTR;
constexpr int L_O = L_ML + 2 * 8 * 8 * 8;
constexpr int L_WT = L_O + 512;
constexpr int L_CTL = L_WT + 256;
constexpr int L_END = L_CTL + 128;
enum { C_DONE = 0, C_FULL = 8, C_QR = 10, C_Q = 11, C_ML = 12, C_OB = 13, C_OBD = 14, C_OA = 15, C_WT = 16, C_O = 17, C_P2 = 18 };
typedef __attribute__((address_space(3))) unsigned lds_u32;
#define CB() asm volatile("" ::: "memory")
__device__ __forceinline__ void arrive(unsigned char* lds, int c, int lane) {
  CB();
  if (lane == 0) __hip_atomic_fetch_add((lds_u32*)(lds + L_CTL) + c, 1u, __ATOMIC_RELAXED, __HIP_MEMORY_SCOPE_WORKGROUP);
  CB();
}
__device__ __forceinline__ void setw(unsigned char* lds, int c, unsigned v) {
  CB();
  __hip_atomic_store((lds_u32*)(lds + L_CTL) + c, v, __ATOMIC_RELAXED, __HIP_MEMORY_SCOPE_WORKGROUP);
  CB();
}
template <int SLEEP> __device__ __forceinline__ void await(unsigned char* lds, int c, unsigned n) {
  CB();
#pragma unroll 1
  for (unsigned spins = 0; spins < LSPIN_CAP; ++spins) {
    if ((unsigned)__builtin_amdgcn_readfirstlane(__hip_atomic_load((lds_u32*)(lds + L_CTL) + c, __ATOMIC_RELAXED, __HIP_MEMORY_SCOPE_WORKGROUP)) >= n) break;
    if (SLEEP) __builtin_amdgcn_s_sleep(SLEEP);
  }
  CB();
}

__device__ __forceinline__ void until(u64 t0, unsigned ticks) {
#pragma unroll 1
  while ((unsigned)(__builtin_amdgcn_s_memrealtime() - t0) < ticks) __builtin_amdgcn_s_sleep(2);
}

constexpr int NSTAMP = 16;
#define STAMP(i)
#define STAMP_OUT()

#define T1 580
#define T2 850
#define DBG 0
#define PRE2 1
#define FM 22
#define S1 10
#define S4 1
#define HOP1EARLY 0
#define WND 12
#define RELOAD() do { __builtin_amdgcn_s_sleep(1); CB(); } while (0)
#define POL "nt"
typedef int v4i __attribute__((ext_vector_type(4)));
__device__ __forceinline__ v4i srd(const void* p, unsigned bytes) { const u64 a = (u64)p; return v4i{(int)(unsigned)a, (int)(unsigned)(a >> 32) & 0xffff, (int)bytes, 0x00020000}; }
__device__ __forceinline__ void dma(int m0, int voff, v4i r, int soff) {
  asm volatile("s_mov_b32 m0, %0\n\ts_nop 0\n\tbuffer_load_dwordx4 %1, %2, %3 offen " POL " lds" :: "s"(m0), "v"(voff), "s"(r), "s"(soff) : "memory");
}
template <int N> __device__ __forceinline__ void landed(int full_addr, int cnt) {
  asm volatile("s_waitcnt vmcnt(%2)\n\tds_write_b32 %0, %1" :: "v"(full_addr), "v"(cnt), "i"(N) : "memory");
}
template <int N, int LO> __device__ __forceinline__ void drain2(int full_addr, int total, int& pub) {
  if constexpr (N >= LO) {
    pub = max(total - N, pub);
    landed<N>(full_addr, pub);
    drain2<N - 2, LO>(full_addr, total, pub);
  }
}
template <int V> struct IC { static constexpr int value = V; };

__global__ void __launch_bounds__(NTHR, 3) fused_decode(Params p) {
  __shared__ __attribute__((aligned(16))) unsigned char lds[L_END];
#define HMARK(i)
  HMARK(0);
  const int tid = threadIdx.x, lane = tid & 63, wv = __builtin_amdgcn_readfirstlane(tid >> 6);
  const int j = blockIdx.x;
  const int G = lane >> 4, c16 = lane & 15;
  const bool hi8 = (lane & 8) != 0;
  const int pair = j >> 4, b = pair >> 2, hkv = pair & 3, s = j & 15;
  const int br = j >> 7, cb = (j & 127) >> 3, ri = (j & 127) >> 1, ch = j & 1;
  const unsigned KVB = 16u * SK * DH * 2, WB = DM * NQ * 2;

  if (__builtin_expect(wv < 2, 1)) {
    __builtin_amdgcn_s_setprio(3);
    const int l = wv;
    const int rot = j * 5 + (j >> 3);
    const int lin = lane * 16;
    const int vk0 = G * 256 + ((c16 ^ (2 * G)) * 16), vk1 = G * 256 + ((c16 ^ (8 + 2 * G)) * 16);
    const int voq = (8 * (lane >> 3)) * (NQ * 2) + (lane & 7) * 16, voo = (8 * (lane >> 3)) * (DM * 2) + (lane & 7) * 16;
    const int lds0 = (int)(unsigned)(size_t)(__attribute__((address_space(3))) unsigned char*)lds;
    const int ring = lds0 + L_RING + l * (NSLOT * 8192), full_addr = lds0 + L_CTL + (C_FULL + l) * 4, done_addr = lds0 + L_CTL + (C_DONE + 4 * l) * 4;
    int f = 0, slot = 0, pub = 0, freed = NSLOT;
    auto fill = [&](auto wc, v4i r, int v0, int v1, int base, int sc, int sh) {
      constexpr int W = decltype(wc)::value;
      if (f >= freed) {
        pub = 8 * f;
        landed<0>(full_addr, pub);
#pragma unroll 1
        for (unsigned spins = 0; spins < LSPIN_CAP; ++spins) {
          v4i d;
          asm volatile("ds_read_b128 %0, %1\n\ts_waitcnt lgkmcnt(0)" : "=v"(d) : "v"(done_addr) : "memory");
          freed = NSLOT + __builtin_amdgcn_readfirstlane(min(min(d[0], d[1]), min(d[2], d[3])));
          if (f < freed) break;
          __builtin_amdgcn_s_sleep(1);
        }
      } else if (f + 2 >= freed) {
        v4i d;
        asm volatile("ds_read_b128 %0, %1\n\ts_waitcnt lgkmcnt(0)" : "=v"(d) : "v"(done_addr) : "memory");
        freed = NSLOT + __builtin_amdgcn_readfirstlane(min(min(d[0], d[1]), min(d[2], d[3])));
      }
      const int dst = ring + slot * 8192;
#pragma unroll
      for (int c = 0; c < 4; ++c) {
        if constexpr (W > 0) { pub = max(8 * f + 2 * c - (W - 2), pub); landed<W - 2>(full_addr, pub); }
        dma(dst + c * 2048, v0, r, base + c * sc);
        dma(dst + c * 2048 + 1024, v1, r, base + c * sc + sh);
      }
      ++f; slot = slot == NSLOT - 1 ? 0 : slot + 1;
    };
    const int baseA = (pair * SK + s * 512 + l * 32) * 256, baseB = (pair * SK + (SK - WIN) + s * 128 + l * 32) * 256;
    {
      const v4i r = srd(br ? p.wqB : p.wqA, WB);
      const int baseQ = (128 * (j & 7) + 64 * l) * (NQ * 2) + cb * 512;
      fill(IC<0>{}, r, voq, voq, baseQ, 128, NQ * 2);
      HMARK(1);
      __builtin_amdgcn_s_barrier();
      HMARK(2);
#pragma unroll 1
      for (int t = 1; t < 4; ++t) fill(IC<WND>{}, r, voq, voq, baseQ + 2 * t * (NQ * 2), 128, NQ * 2);
    }
    STAMP(1);
    HMARK(3);
    {
      const v4i r = srd(p.kA, KVB);
#pragma unroll 1
      for (int t = 0; t < 8; ++t) fill(IC<WND>{}, r, vk0, vk1, baseA + (((t + rot) & 7) << 14), 2048, 1024);
    }
    {
      const v4i r = srd(p.kB, KVB);
#pragma unroll 1
      for (int t = 0; t < 2; ++t) fill(IC<WND>{}, r, vk0, vk1, baseB + (t << 14), 2048, 1024);
    }
    STAMP(2);
    {
      const v4i r = srd(p.vB, KVB);
#pragma unroll 1
      for (int t = 0; t < 2; ++t) fill(IC<WND>{}, r, lin, lin, baseB + (t << 14), 2048, 1024);
    }
    {
      const v4i r = srd(p.vA, KVB);
#pragma unroll 1
      for (int t = 0; t < 8; ++t) fill(IC<WND>{}, r, lin, lin, baseA + (((t + rot) & 7) << 14), 2048, 1024);
    }
    STAMP(3);
    HMARK(4);
    {
      const v4i r = srd(br ? p.woB : p.woA, WB);
      const int baseO = (64 * ri) * (DM * 2) + (512 * ch + 256 * l) * 2;
#pragma unroll 1
      for (int t = 0; t < 4; ++t) fill(IC<WND>{}, r, voo, voo, baseO + 2 * t * (DM * 2), 128, DM * 2);
    }
    STAMP(4);
    drain2<WND - 2, 0>(full_addr, 8 * 28, pub);
    HMARK(5);
    STAMP(5);
    STAMP_OUT();
    return;
  }

  const rsrc_t rws = mk(p.ws, WS_END);
  const f4 z = {0.f, 0.f, 0.f, 0.f};
  if (wv == 2) {
    __builtin_amdgcn_s_setprio(2);
    const u64 t0 = __builtin_amdgcn_s_memrealtime();
    if (lane < 32) *(unsigned*)(lds + L_CTL + lane * 4) = 0u;
    asm volatile("s_waitcnt lgkmcnt(0)" ::: "memory");
    __builtin_amdgcn_s_barrier();
    const u64 tag = launch_tag(p.ws);
    const unsigned tl = (unsigned)(tag >> (16 * (lane & 3)));
    await<2>(lds, C_QR, 8);
    {
      const int un = 2 * (lane & 3) + (hi8 ? 1 : 0);
      const unsigned tu = (unsigned)(tag >> (16 * (un & 3)));
#pragma unroll
      for (int u = 0; u < 4; ++u)
        stc(mark(*(const f4*)(lds + L_RED + u * 1024 + lane * 16) + *(const f4*)(lds + L_RED + (u + 4) * 1024 + lane * 16), tu), rws,
            ((lane >> 2) & 1) * 4096 + G * 128 + un * 16, WS_Q1 + ((j & ~7) + 2 * u) * 4096 + (j & 7) * 512);
    }
    STAMP(3);
    until(t0, T1);
    {
      v4u g[4];
      gather2<4, S1>(g, tl, lane, rws, lane * 16, WS_Q1 + j * 4096, 0);
      const f4 r = (val(g[0]) + val(g[1])) + (val(g[2]) + val(g[3]));
      const unsigned r01 = pk2(red32(r[0], r[0]), red32(r[1], r[1])), r23 = pk2(red32(r[2], r[2]), red32(r[3], r[3]));
      if (lane < 32)
        stc(mark(f4{uf(r01 << 16), uf(r01 & 0xffff0000u), uf(r23 << 16), uf(r23 & 0xffff0000u)}, tl), rws,
            (lane >> 3) * (NQ * 4) + (lane & 7) * 16, WS_QP + (br * 4 * NQ + 32 * (j & 127)) * 4);
    }
    STAMP(4);
    until(t0, T2);
    {
      v4u g[8];
      gather2<8, S1>(g, tl, lane, rws, lane * 16, WS_QP + (b * NQ + hkv * 1024) * 4, 4 * NQ * 4);
#pragma unroll
      for (int u = 0; u < 8; ++u)
        *(v2u*)(lds + L_Q + (2 * u + (lane >> 5)) * QSTR + (lane & 31) * 8) =
            v2u{__builtin_amdgcn_perm(g[u][1], g[u][0], 0x07060302u), __builtin_amdgcn_perm(g[u][3], g[u][2], 0x07060302u)};
    }
    setw(lds, C_Q, 1u);
    STAMP(5);
    await<2>(lds, C_ML, 8);
    {
      f4 mv[4];
#pragma unroll
      for (int u = 0; u < 4; ++u) mv[u] = *(const f4*)(lds + L_ML + c16 * 64 + u * 16);
      float M = -INFINITY, L = 0.f;
#pragma unroll
      for (int u = 0; u < 4; ++u) M = fmaxf(M, fmaxf(mv[u][0], mv[u][2]));
#pragma unroll
      for (int u = 0; u < 4; ++u) { L = fmaf(__builtin_amdgcn_exp2f(mv[u][0] - M), mv[u][1], L); L = fmaf(__builtin_amdgcn_exp2f(mv[u][2] - M), mv[u][3], L); }
      if (lane < 16) stc(v4u{fu(M), (unsigned)tag, fu(L), (unsigned)(tag >> 32)}, rws, (hi8 ? 256 * 128 : 0) + (lane & 7) * 16, WS_ML + j * 128);
    }
    STAMP(6);
    STAMP(7);
    await<4>(lds, C_FULL, 8 * FM);
    {
      const int mo = (G * 4 * 16 + c16) * 128, mg = WS_ML + ((br * 16 + (ri >> 4)) * 16) * 128 + ((ri >> 1) & 7) * 16;
      v4u ml = ldc(rws, mo, mg);
#pragma unroll 1
      for (unsigned spins = 0; spins < SPIN_CAP; ++spins) {
        const bool bad = ml[1] != (unsigned)tag || ml[3] != (unsigned)(tag >> 32);
        if (!wave_bad(bad)) break;
        __builtin_amdgcn_s_sleep(S1); CB();
        const v4u n = ldc(rws, bad ? mo : 0x7ffffff0, mg);
        ml = v4u{bad ? n[0] : ml[0], bad ? n[1] : ml[1], bad ? n[2] : ml[2], bad ? n[3] : ml[3]};
      }
      const float m = uf(ml[0]);
      float M = m;
      M = fmaxf(M, __shfl_xor(M, 1)); M = fmaxf(M, __shfl_xor(M, 2)); M = fmaxf(M, __shfl_xor(M, 4)); M = fmaxf(M, ror8(M));
      const float w0 = __builtin_amdgcn_exp2f(m - M);
      float L = w0 * uf(ml[2]);
      L += __shfl_xor(L, 1); L += __shfl_xor(L, 2); L += __shfl_xor(L, 4); L += ror8(L);
      *(float*)(lds + L_WT + (G * 8 + (lane & 7)) * 8 + (hi8 ? 4 : 0)) = w0 / L;
      setw(lds, C_WT, 1u);
    }
    STAMP(8);
    await<1>(lds, C_P2, 8);
    STAMP(9);
    {
      v4u g[8];
      gather2<8, 0>(g, tl, lane, rws, lane * 16, WS_P2 + ((br * 2 + ch) * 64 + ri) * (64 * 128), 4096);
      f4 v = val(g[0]);
#pragma unroll
      for (int u = 1; u < 8; ++u) v += val(g[u]);
      const float y0 = red32(v[0], v[2]), y1 = red32(v[1], v[3]);
      float y = red16(y0, y1);
      y = y + ror8(y);
      if (!hi8) {
        const int pc = lane & 7;
        ((__bf16*)p.out)[(pc >> 1) * 2048 + br * 1024 + 512 * ch + 8 * ri + 4 * (pc & 1) + 2 * (lane >> 5) + (G & 1)] = (__bf16)y;
      }
      if (j == 0 && lane == 0) __builtin_amdgcn_raw_buffer_store_b64(v2u{(unsigned)tag, (unsigned)(tag >> 32)}, rws, 0, WS_EPOCH, AUX_SC1);
    }
    STAMP(10);
    STAMP_OUT();
    return;
  }

  const int w = wv - 3;
  const v4u xa = __builtin_amdgcn_raw_buffer_load_b128(mk(br ? p.xB : p.xA, 4 * DM * 2), ((c16 >> 2) * DM + 8 * (2 * G + (c16 & 1))) * 2,
                                                       (128 * (j & 7) + 64 * (w >> 2)) * 2, 0);
  const unsigned char* mine = lds + L_RING + (w >> 2) * (NSLOT * 8192) + (w & 3) * 2048;
  const int kr0 = (lane & 7) * 256 + (((hi8 ? 4 : 0) + G) ^ (2 * (lane & 7))) * 16;
  const int vr0 = (4 * (G & 1) + (G >> 1)) * 256 + c16 * 16;
  __builtin_amdgcn_s_barrier();
  const u64 tag = launch_tag(p.ws);
#define RD(f, o) (*(const v4u*)(mine + ((f) % NSLOT) * 8192 + (o)))
#define TAKE(f, N, dst, T, O0, O1, SL) do { await<SL>(lds, C_FULL + (w >> 2), 8 * ((f) + (N) - 1) + 2 * (w & 3) + 2); \
    _Pragma("unroll") for (int u_ = 0; u_ < (N); ++u_) { const int t_ = (T) + u_; (void)t_; dst[2 * t_] = RD((f) + u_, O0); dst[2 * t_ + 1] = RD((f) + u_, O1); } \
    asm volatile("s_waitcnt lgkmcnt(0)" ::: "memory"); setw(lds, C_DONE + w, (f) + (N)); \
    _Pragma("unroll") for (int u_ = 0; u_ < 2 * (N); ++u_) asm volatile("" : "+v"(dst[2 * (T) + u_])); } while (0)
#define TAKEK(f, N, dst, T) TAKE(f, N, dst, T, kr0 ^ ((t_ & 1) * 64), kr0 ^ ((t_ & 1) * 64) ^ 128, 2)
#define TAKEV(f, N, dst, T, SL) TAKE(f, N, dst, T, vr0, vr0 + 512, SL)
#define TAKEW(f, N, dst, T, SL) TAKE(f, N, dst, T, lane * 16, lane * 16 + 1024, SL)
  v4u kB[4], vB[4], kA[16], vt[8], wt[8];
  {
    f4 d[8] = {z, z, z, z, z, z, z, z};
    TAKEW(0, 4, vt, 0, 1);
    tr64(vt, wt); tile64_acc(wt, xa, d);
    *(f4*)(lds + L_RED + w * 1024 + lane * 16) = tile64_fin(d, hi8);
    arrive(lds, C_QR, lane);
  }
  STAMP(1);
  TAKEK(4, 2, kA, 0); TAKEK(6, 2, kA, 2); TAKEK(8, 2, kA, 4); TAKEK(10, 2, kA, 6); TAKEK(12, 2, kB, 0);
  TAKEV(14, 2, vB, 0, 2);
  TAKEV(16, 2, vt, 0, 2);
  STAMP(2);
  await<2>(lds, C_Q, 1);
  STAMP(3);
  constexpr float C2 = 0.088388347648318440f * 1.4426950408889634f;
  unsigned pA[4], pB;
  float* red = (float*)(lds + L_RED) + (w * 8 + (lane & 7)) * RSTR + 32 * G;
  {
    const unsigned char* qp = lds + L_Q + (lane & 7) * QSTR + (hi8 ? 64 : 0) + G * 16;
    const bool t0lane = (G >> 1) == (c16 >> 3);
    float za[8], zb[2];
    const v4u qa0 = *(const v4u*)qp, qa1 = *(const v4u*)(qp + 128), qb0 = *(const v4u*)(qp + 8 * QSTR), qb1 = *(const v4u*)(qp + 8 * QSTR + 128);
#pragma unroll
    for (int u = 0; u < 4; ++u) qk16(kA + 4 * u, qa0, qa1, t0lane, za[2 * u], za[2 * u + 1]);
    qk16(kB, qb0, qb1, t0lane, zb[0], zb[1]);
    float mA = fmaxf(fmaxf(fmaxf(za[0], za[1]), fmaxf(za[2], za[3])), fmaxf(fmaxf(za[4], za[5]), fmaxf(za[6], za[7])));
    mA = max_x8_16_32(mA) * C2;
    const float mB = max_x8_16_32(fmaxf(zb[0], zb[1])) * C2;
    float lA = 0.f;
#pragma unroll
    for (int u = 0; u < 8; ++u) { za[u] = __builtin_amdgcn_exp2f(fmaf(za[u], C2, -mA)); lA += za[u]; }
    zb[0] = __builtin_amdgcn_exp2f(fmaf(zb[0], C2, -mB)); zb[1] = __builtin_amdgcn_exp2f(fmaf(zb[1], C2, -mB));
    lA = sum_x8_16_32(lA);
    const float lB = sum_x8_16_32(zb[0] + zb[1]);
    if (lane < 16) *(float2*)(lds + L_ML + (lane * 8 + w) * 8) = hi8 ? make_float2(mB, lB) : make_float2(mA, lA);
    arrive(lds, C_ML, lane);
    await<1>(lds, C_ML, 8);
    float MA = -INFINITY, MB = -INFINITY;
#pragma unroll
    for (int u = 0; u < 4; ++u) {
      const f4 ma = *(const f4*)(lds + L_ML + (lane & 7) * 64 + u * 16), mb = *(const f4*)(lds + L_ML + (8 + (lane & 7)) * 64 + u * 16);
      MA = fmaxf(MA, fmaxf(ma[0], ma[2])); MB = fmaxf(MB, fmaxf(mb[0], mb[2]));
    }
    const float fA = __builtin_amdgcn_exp2f(mA - MA), fB = __builtin_amdgcn_exp2f(mB - MB);
#pragma unroll
    for (int u = 0; u < 4; ++u) pA[u] = pk2(za[2 * u] * fA, za[2 * u + 1] * fA);
    pB = pk2(zb[0] * fB, zb[1] * fB);
  }
  STAMP(4);
  const unsigned tl = (unsigned)(tag >> (16 * (lane & 3)));
  const float* rd = (const float*)(lds + L_RED) + (2 * (w & 3) + (lane >> 5)) * RSTR + (lane & 31) * 4;
  const int opv = (lane >> 5) * (16 * 512) + (lane & 31) * 16, ops = WS_OP + (((w >> 2) * 4 + b) * 32 + hkv * 8 + 2 * (w & 3)) * (16 * 512) + s * 512;
  {
    const v2u pb = {pB, ror8u(pB)};
    f4 o[8];
    o[0] = mfma16(tr4<0>(vB), pb, z); o[1] = mfma16(tr4<1>(vB), pb, z); o[2] = mfma16(tr4<2>(vB), pb, z); o[3] = mfma16(tr4<3>(vB), pb, z);
    o[4] = mfma16(tr4<4>(vB), pb, z); o[5] = mfma16(tr4<5>(vB), pb, z); o[6] = mfma16(tr4<6>(vB), pb, z); o[7] = mfma16(tr4<7>(vB), pb, z);
    if (!hi8) put_o(red, o);
    arrive(lds, C_OB, lane);
  }
  if (w >= 4) {
    await<1>(lds, C_OB, 8);
    f4 r = *(const f4*)rd;
#pragma unroll
    for (int u = 1; u < 8; ++u) r += *(const f4*)(rd + u * 8 * RSTR);
    stc(mark(r, tl), rws, opv, ops);
    arrive(lds, C_OBD, lane);
  }
  TAKEV(18, 2, vt, 2, 1);
  f4 o[8] = {z, z, z, z, z, z, z, z};
  pv32(vt, v4u{pA[0], ror8u(pA[0]), pA[1], ror8u(pA[1])}, o);
#pragma unroll
  for (int u = 0; u < 8; ++u) asm volatile("" : "+v"(o[u]));
  STAMP(5);
  TAKEV(20, 3, vt, 0, 1);
  await<1>(lds, C_OBD, 4);
  TAKEV(23, 1, vt, 3, 0);
  STAMP(6);
  pv32(vt, v4u{pA[2], ror8u(pA[2]), pA[3], ror8u(pA[3])}, o);
  if (!hi8) put_o(red, o);
  arrive(lds, C_OA, lane);
  if (w < 4) {
    await<0>(lds, C_OA, 8);
    f4 r = *(const f4*)rd;
#pragma unroll
    for (int u = 1; u < 8; ++u) r += *(const f4*)(rd + u * 8 * RSTR);
    stc(mark(r, tl), rws, opv, ops);
  }
  STAMP(7);
  TAKEW(24, 2, vt, 0, 1);
  TAKEW(26, 2, vt, 2, 0);
  {
    const int og = WS_OP + ((br * 4 + (w >> 1)) * 32 + (ri >> 1)) * (16 * 512) + (64 * (ri & 1) + 32 * (w & 1)) * 4;
    const int ov = (lane >> 3) * 512 + (lane & 7) * 16, o1 = 8 * 512;
    v4u g0 = ldc(rws, ov, og), g1 = ldc(rws, ov, og + o1);
    PIN();
    STAMP(8);
    tr64(vt, wt);
#pragma unroll
    for (int u = 0; u < 8; ++u) asm volatile("" : "+v"(wt[u]));
    await<1>(lds, C_WT, 1);
    const float2 wgt = *(const float2*)(lds + L_WT + ((w >> 1) * 8 + (lane >> 3)) * 8);
    PIN();
#pragma unroll 1
    for (unsigned spins = 0; spins < SPIN_CAP; ++spins) {
      if (!wave_bad((mis(g0, tl) | mis(g1, tl)) != 0u)) break;
      __builtin_amdgcn_s_sleep(S4); CB();
      repoll(g0, tl, lane, rws, ov, og);
      repoll(g1, tl, lane, rws, ov, og + o1);
    }
    STAMP(9);
    const f4 v = val(g0) * wgt.x + val(g1) * wgt.y;
    const float y0 = red32(v[0], v[2]), y1 = red32(v[1], v[3]);
    float y = red16(y0, y1);
    y = y + ror8(y);
    *(__bf16*)(lds + L_O + ((w >> 1) * 64 + 32 * (w & 1) + 4 * (lane & 7) + 2 * (lane >> 5) + (G & 1)) * 2) = (__bf16)y;
  }
  arrive(lds, C_O, lane);
  await<0>(lds, C_O, 8);
  {
    const v4u a = *(const v4u*)(lds + L_O + ((c16 >> 2) * 64 + 8 * (2 * G + (c16 & 1))) * 2);
    f4 d[8] = {z, z, z, z, z, z, z, z};
    tile64_acc(wt, a, d);
    stc(mark(tile64_fin(d, hi8), (unsigned)(tag >> (16 * ((2 * G + (hi8 ? 1 : 0)) & 3)))), rws, (lane & 7) * (64 * 128) + G * 32 + (hi8 ? 16 : 0),
        WS_P2 + (((br * 2 + ch) * 64 + 8 * w) * 64 + ri) * 128);
  }
  arrive(lds, C_P2, lane);
  STAMP(10);
  STAMP_OUT();
}

extern "C" void kernel_launch(void* const* d_in, const int* in_sizes, int n_in, void* d_out, int out_size, void* d_ws, size_t ws_size,
                              hipStream_t stream) {
  Params p;
  p.xA = d_in[0]; p.kA = d_in[1]; p.vA = d_in[2]; p.wqA = d_in[3]; p.woA = d_in[4];
  p.xB = d_in[5]; p.kB = d_in[6]; p.vB = d_in[7]; p.wqB = d_in[8]; p.woB = d_in[9];
  p.out = d_out; p.ws = d_ws;
  fused_decode<<<NWG, NTHR, 0, stream>>>(p);
}
```

```cpp
#define AMPL 3
#include <hip/hip_runtime.h>
#include <hip/hip_bf16.h>
#include <stdint.h>

typedef unsigned v4u __attribute__((ext_vector_type(4)));
typedef unsigned v2u __attribute__((ext_vector_type(2)));
typedef int v4i __attribute__((ext_vector_type(4)));
typedef float f4 __attribute__((ext_vector_type(4)));
typedef __bf16 bf8 __attribute__((ext_vector_type(8)));
typedef short s4 __attribute__((ext_vector_type(4)));
typedef __amdgpu_buffer_rsrc_t rsrc_t;
typedef unsigned long long u64;
typedef __attribute__((address_space(3))) unsigned lds_u32;

#define STAMPS 0
#define DBG 0
#define WND 10
#define WHI 22
#define FW 24
#define TL 1410
#define GI 27
#define PREP 2
#define TPD 75
#define TQS 520
#define TG 1650
#define D2 15
#define S1 8
#define S4 1
#define S5 0
#define KSL 2
#define OAUX 16
#define QX 0
#define TQG 0
#define QG 0
#define ILV 2
#define TOUCH 0
#define CPF 3296
#define ADP 415
#define ADP0 0
#define AMX 60
#define OPK 0
#define EPE 0
#define STG 0
#define TK 2

constexpr int NWG = 256, NTHR = 640;
constexpr int SK = 8192, WIN = 2048, DH = 128, DM = 1024, NQ = 4096;
constexpr unsigned WS_EPOCH = 0;
constexpr unsigned WS_QP = 4096;
constexpr unsigned WS_OP = WS_QP + 2 * 4 * 4 * 4096 * 4;
constexpr unsigned WS_PAD = WS_OP + 2 * 4 * 32 * 16 * 512;
constexpr unsigned WS_P2 = WS_PAD + 8192;
constexpr unsigned WS_END = WS_P2 + 2 * 2 * 64 * 64 * 128;
constexpr unsigned WS_STAMP = WS_END + 8192;
constexpr int AUX_SC1 = 16, OOB = 0x7ffffff0;
constexpr unsigned SPIN_CAP = 1u << 21, LSPIN_CAP = 1u << 23;
constexpr int NSLOT = 9;
constexpr int RING = NSLOT * 8192;
constexpr int L_QR = 2 * RING;
constexpr int L_Q = L_QR + 10240;
constexpr int QSTR = 272;
constexpr int L_ML = L_Q + 2 * 8 * QSTR;
constexpr int L_O = L_ML + 2 * 8 * 8 * 8;
constexpr int L_CTL = L_O + 512;
constexpr int L_END = L_CTL + 128;
enum { C_DONE = 0, C_FULL = 8, C_QR = 10, C_Q = 11, C_ML = 12, C_P = 13, C_O = 14, C_TG = 16  , C_F = 17, C_ISS = 18   };

struct Params {
  const void *wqA, *wqB, *kA, *kB, *vB, *vA, *woA, *woB, *xA, *xB;
  void *out, *ws;
};

#define PIN() __builtin_amdgcn_sched_barrier(0)
#define CB() asm volatile("" ::: "memory")
__device__ __forceinline__ rsrc_t mk(const void* p, unsigned bytes) { return __builtin_amdgcn_make_buffer_rsrc((void*)p, 0, (int)bytes, 0x00020000); }
__device__ __forceinline__ v4u ldc(rsrc_t r, int voff, int soff) { return __builtin_amdgcn_raw_buffer_load_b128(r, voff, soff, AUX_SC1); }
__device__ __forceinline__ void stc(v4u v, rsrc_t r, int voff, int soff) {
  __builtin_amdgcn_raw_buffer_store_b128(v, r, voff, soff, AUX_SC1);
  asm volatile("s_nop 1" :: "v"(v) : "memory");
}
__device__ __forceinline__ f4 mfma32(v4u a, v4u b, f4 c) { return __builtin_amdgcn_mfma_f32_16x16x32_bf16(__builtin_bit_cast(bf8, a), __builtin_bit_cast(bf8, b), c, 0, 0, 0); }
__device__ __forceinline__ unsigned fu(float x) { return __builtin_bit_cast(unsigned, x); }
__device__ __forceinline__ float uf(unsigned x) { return __builtin_bit_cast(float, x); }
__device__ __forceinline__ unsigned ror8u(unsigned x) { return (unsigned)__builtin_amdgcn_mov_dpp((int)x, 0x128, 0xf, 0xf, true); }
__device__ __forceinline__ float ror8(float x) { return uf(ror8u(fu(x))); }
__device__ __forceinline__ unsigned pk2(float lo, float hi) {
  typedef __bf16 bf2 __attribute__((ext_vector_type(2)));
  typedef float f2 __attribute__((ext_vector_type(2)));
  f2 v = {lo, hi};
  return __builtin_bit_cast(unsigned, __builtin_convertvector(v, bf2));
}
template <int T> __device__ __forceinline__ unsigned tr(const v4u& a, const v4u& b) {
  return __builtin_amdgcn_perm(b[T >> 1], a[T >> 1], (T & 1) ? 0x07060302u : 0x05040100u);
}
template <int T> __device__ __forceinline__ v4u tr8(const v4u* r) { return v4u{tr<T>(r[0], r[1]), tr<T>(r[2], r[3]), tr<T>(r[4], r[5]), tr<T>(r[6], r[7])}; }
__device__ __forceinline__ void tr64(const v4u* wr, v4u* wt) {
  wt[0] = tr8<0>(wr); wt[1] = tr8<1>(wr); wt[2] = tr8<2>(wr); wt[3] = tr8<3>(wr);
  wt[4] = tr8<4>(wr); wt[5] = tr8<5>(wr); wt[6] = tr8<6>(wr); wt[7] = tr8<7>(wr);
}
__device__ __forceinline__ float red32(float a, float b) { auto s = __builtin_amdgcn_permlane32_swap(fu(a), fu(b), false, false); return uf(s[0]) + uf(s[1]); }
__device__ __forceinline__ float red16(float a, float b) { auto s = __builtin_amdgcn_permlane16_swap(fu(a), fu(b), false, false); return uf(s[0]) + uf(s[1]); }
__device__ __forceinline__ float mx(float a, float b) { float r; asm("v_max_f32 %0, %1, %2" : "=v"(r) : "v"(a), "v"(b)); return r; }
__device__ __forceinline__ float max_x8_16_32(float x) {
  x = mx(x, ror8(x));
  auto s = __builtin_amdgcn_permlane16_swap(fu(x), fu(x), false, false); x = mx(uf(s[0]), uf(s[1]));
  auto t = __builtin_amdgcn_permlane32_swap(fu(x), fu(x), false, false); return mx(uf(t[0]), uf(t[1]));
}
__device__ __forceinline__ float sum_x8_16_32(float x) { x = x + ror8(x); x = red16(x, x); return red32(x, x); }
__device__ __forceinline__ float fold4(f4 v) {
  const float y = red16(red32(v[0], v[2]), red32(v[1], v[3]));
  return y + ror8(y);
}
__device__ __forceinline__ float sel(bool c, float a, float b) { asm volatile("" : "+v"(a), "+v"(b)); return c ? a : b; }
__device__ __forceinline__ f4 tile64(const v4u* wt, v4u a, bool hi8) {
  const f4 z = {0.f, 0.f, 0.f, 0.f};
  f4 d[8], r;
#pragma unroll
  for (int t = 0; t < 4; ++t) { d[t] = mfma32(a, wt[t], z); d[t + 4] = mfma32(a, wt[t + 4], z); }
#pragma unroll
  for (int u = 0; u < 4; ++u) r[u] = sel(hi8, d[4 + u][1], d[u][0]) + ror8(sel(hi8, d[u][1], d[4 + u][0]));
  return r;
}
__device__ __forceinline__ void qk_fin(const f4& da, const f4& db, bool t0lane, float& z0, float& z1) {
  const float x0 = t0lane ? da[0] : db[0], x1 = t0lane ? da[1] : db[1], x2 = t0lane ? da[2] : db[2], x3 = t0lane ? da[3] : db[3];
  auto s = __builtin_amdgcn_permlane32_swap(fu(x0), fu(x1), false, false);
  auto t = __builtin_amdgcn_permlane32_swap(fu(x2), fu(x3), false, false);
  const unsigned r1 = pk2(uf(s[0]) + ror8(uf(s[1])), uf(t[0]) + ror8(uf(t[1])));
  const unsigned r2 = pk2(uf(r1 << 16) * 0.088388347648318440f, uf(r1 & 0xffff0000u) * 0.088388347648318440f);
  z0 = uf(r2 << 16);
  z1 = uf(r2 & 0xffff0000u);
}
__device__ __forceinline__ v4u mark(f4 v, unsigned tf) {
  const v4u u = __builtin_bit_cast(v4u, v);
  return v4u{(u[0] & ~15u) | (tf & 15u), (u[1] & ~15u) | ((tf >> 4) & 15u), (u[2] & ~15u) | ((tf >> 8) & 15u), (u[3] & ~15u) | ((tf >> 12) & 15u)};
}
__device__ __forceinline__ unsigned mis(const v4u& g, unsigned tf) { return ((g[0] ^ tf) | (g[1] ^ (tf >> 4)) | (g[2] ^ (tf >> 8)) | (g[3] ^ (tf >> 12))) & 15u; }
__device__ __forceinline__ f4 val(const v4u& g) { return __builtin_bit_cast(f4, g & ~15u); }
__device__ __forceinline__ v4u mark8(f4 v, unsigned e, unsigned o) {
  const v4u u = __builtin_bit_cast(v4u, v);
  return v4u{(u[0] & ~255u) | (e & 255u), (u[1] & ~255u) | (o & 255u), (u[2] & ~255u) | (e >> 8), (u[3] & ~255u) | (o >> 8)};
}
__device__ __forceinline__ f4 val8(const v4u& g) { return __builtin_bit_cast(f4, g & ~255u); }
__device__ __forceinline__ unsigned side16(const v4u& g) {
  const unsigned e = __builtin_amdgcn_perm(g[2], g[0], 0x0c0c0400u), o = __builtin_amdgcn_perm(g[3], g[1], 0x0c0c0400u);
  return ((e >> 4) & 0x0f0fu) | (o & 0xf0f0u);
}
__device__ __forceinline__ float lse8(const v4u& g, unsigned xs) {
  const unsigned x = side16(g);
  const unsigned y = (unsigned)__builtin_amdgcn_mov_dpp((int)x, 0xB1, 0xf, 0xf, true);
  return uf(__builtin_amdgcn_perm(x, y, xs));
}
__device__ __forceinline__ bool wave_bad(bool bad) { return __builtin_amdgcn_ballot_w64(bad) != 0ull; }
template <int N, int SLEEP> __device__ __forceinline__ unsigned settle(v4u* g, unsigned tf, int lane, rsrc_t r, int voff, int soff, int stride) {
  unsigned spins = 0;
  soff = __builtin_amdgcn_readfirstlane(soff);
#pragma unroll 1
  for (; spins < SPIN_CAP; ++spins) {
    unsigned m[N], any = 0;
#pragma unroll
    for (int u = 0; u < N; ++u) { m[u] = mis(g[u], tf); any |= m[u]; }
    if (__builtin_expect(!wave_bad(any != 0u), 1)) break;
    if (SLEEP) __builtin_amdgcn_s_sleep(SLEEP);
    CB();
    bool bad[N];
    v4u n[N];
#pragma unroll
    for (int u = 0; u < N; ++u) {
      bad[u] = ((__builtin_amdgcn_ballot_w64(m[u] != 0u) >> (lane & 56)) & 0xffull) != 0ull;
      n[u] = ldc(r, bad[u] ? voff : OOB, soff + u * stride);
    }
    PIN();
#pragma unroll
    for (int u = 0; u < N; ++u) g[u] = v4u{bad[u] ? n[u][0] : g[u][0], bad[u] ? n[u][1] : g[u][1], bad[u] ? n[u][2] : g[u][2], bad[u] ? n[u][3] : g[u][3]};
  }
  return spins;
}
template <int SLEEP> __device__ __forceinline__ void settle2(v4u* g, const unsigned* tf, int lane, rsrc_t r, const int* voff, int soff) {
  soff = __builtin_amdgcn_readfirstlane(soff);
#pragma unroll 1
  for (unsigned spins = 0; spins < SPIN_CAP; ++spins) {
    const unsigned m[2] = {mis(g[0], tf[0]), mis(g[1], tf[1])};
    if (__builtin_expect(!wave_bad((m[0] | m[1]) != 0u), 1)) break;
    if (SLEEP) __builtin_amdgcn_s_sleep(SLEEP);
    CB();
    bool bad[2];
    v4u n[2];
#pragma unroll
    for (int u = 0; u < 2; ++u) {
      bad[u] = ((__builtin_amdgcn_ballot_w64(m[u] != 0u) >> (lane & 56)) & 0xffull) != 0ull;
      n[u] = ldc(r, bad[u] ? voff[u] : OOB, soff);
    }
    PIN();
#pragma unroll
    for (int u = 0; u < 2; ++u) g[u] = v4u{bad[u] ? n[u][0] : g[u][0], bad[u] ? n[u][1] : g[u][1], bad[u] ? n[u][2] : g[u][2], bad[u] ? n[u][3] : g[u][3]};
  }
}
__device__ __forceinline__ u64 launch_tag(const void* ws) {
  u64 e;
  asm volatile("s_load_dwordx2 %0, %1, 0x0\n\ts_waitcnt lgkmcnt(0)" : "=s"(e) : "s"(ws) : "memory");
  u64 d = (e + 0x9E3779B97F4A7C15ull) * 0xBF58476D1CE4E5B9ull;
  d ^= d >> 29;
  return e ^ (d | 0x1111111111111111ull);
}
__device__ __forceinline__ unsigned arrive(unsigned char* lds, int c, int lane) {
  unsigned old = 0;
  CB();
  if (lane == 0) old = __hip_atomic_fetch_add((lds_u32*)(lds + L_CTL) + c, 1u, __ATOMIC_RELAXED, __HIP_MEMORY_SCOPE_WORKGROUP);
  CB();
  return old;
}
__device__ __forceinline__ void setw(unsigned char* lds, int c, unsigned v) {
  CB();
  __hip_atomic_store((lds_u32*)(lds + L_CTL) + c, v, __ATOMIC_RELAXED, __HIP_MEMORY_SCOPE_WORKGROUP);
  CB();
}
template <int SLEEP> __device__ __forceinline__ void await(unsigned char* lds, int c, unsigned n) {
  CB();
#pragma unroll 1
  for (unsigned spins = 0; spins < LSPIN_CAP; ++spins) {
    if (__builtin_amdgcn_readfirstlane(__hip_atomic_load((lds_u32*)(lds + L_CTL) + c, __ATOMIC_RELAXED, __HIP_MEMORY_SCOPE_WORKGROUP)) >= (int)n) break;
    if (SLEEP) __builtin_amdgcn_s_sleep(SLEEP);
  }
  CB();
}
template <int SLEEP> __device__ __forceinline__ void await_both(unsigned char* lds, int n) {
  CB();
#pragma unroll 1
  for (unsigned spins = 0; spins < LSPIN_CAP; ++spins) {
    const u64 v = __hip_atomic_load((__attribute__((address_space(3))) u64*)(lds + L_CTL + C_FULL * 4), __ATOMIC_RELAXED, __HIP_MEMORY_SCOPE_WORKGROUP);
    if (__builtin_amdgcn_readfirstlane(min((int)(unsigned)v, (int)(unsigned)(v >> 32))) >= n) break;
    if (SLEEP) __builtin_amdgcn_s_sleep(SLEEP);
  }
  CB();
}
__device__ __forceinline__ v2u trd(const unsigned char* p) {
  return __builtin_bit_cast(v2u, __builtin_amdgcn_ds_read_tr16_b64_v4i16((__attribute__((address_space(3))) s4*)p));
}
__device__ __forceinline__ v4i srd(const void* p, unsigned bytes) { const u64 a = (u64)p; return v4i{(int)(unsigned)a, (int)(unsigned)(a >> 32) & 0xffff, (int)bytes, 0x00020000}; }
#define DMW_ "s_waitcnt vmcnt(%9)\n\ts_cbranch_scc1 1f\n\ts_waitcnt vmcnt(%8)\n1:\n\t"
#define DMA4_(PA, PB) asm volatile("s_mov_b32 m0, %1\n\ts_cmp_lg_u32 %10, 0\n\tv_add_u32 %0, 2, %0\n\t" DMW_ PA "buffer_load_dwordx4 %2, %4, %5 offen nt lds\n\t" \
    DMW_ PB "buffer_load_dwordx4 %3, %4, %5 offen offset:1024 nt lds\n\t" \
    "v_add_u32 %0, 2, %0\n\t" DMW_ PA "buffer_load_dwordx4 %2, %4, %6 offen offset:2048 nt lds\n\t" \
    DMW_ PB "buffer_load_dwordx4 %3, %4, %6 offen offset:3072 nt lds" \
    : "+v"(cnt) : "s"(m0), "v"(v0), "v"(v1), "s"(r), "s"(oa), "s"(ob), "v"(addr), "i"(N), "i"(NW), "s"(wide) : "scc", "memory")
template <int N, int NW> __device__ __forceinline__ void dma4(int m0, int v0, int v1, v4i r, int oa, int ob, int addr, int& cnt, int wide) {
  if constexpr (N & 1) DMA4_("", "ds_write_b32 %7, %0\n\t");
  else DMA4_("ds_write_b32 %7, %0\n\t", "");
}
__device__ __forceinline__ void publish(int addr, int cnt) { asm volatile("ds_write_b32 %0, %1" :: "v"(addr), "v"(cnt) : "memory"); }

constexpr int NSTAMP = 16;
#define STAMP(i)
#define STAMP_OUT()

__global__ void __launch_bounds__(NTHR, 3) fused_decode(Params p) {
  __shared__ __attribute__((aligned(16))) unsigned char lds[L_END];
  const int tid = threadIdx.x, lane = tid & 63, wv = __builtin_amdgcn_readfirstlane(tid >> 6);
  const int j = blockIdx.x;
  const int G = lane >> 4, c16 = lane & 15;
#define PAIR(j) ((j) & 15)
#define SPLIT(j) ((j) >> 4)
#define RI(j) ((((j) >> 1) & 56) | ((j) & 7))
#define CH(j) (((j) >> 3) & 1)
  const int pair = PAIR(j), s = SPLIT(j);
  const int br = j >> 7, qh = ((j & 7) << 2) | ((j >> 3) & 3), kq = (j >> 5) & 3, ri = RI(j), ch = CH(j);
  const unsigned rot = (unsigned)j * 2654435761u >> 16;
  const unsigned KVB = 16u * SK * DH * 2, WB = DM * NQ * 2;

  const int lds0 = (int)(unsigned)(size_t)(__attribute__((address_space(3))) unsigned char*)lds;
  if (wv >= 2) {
  const int w = wv - 2;
  const u64 tc0 = __builtin_amdgcn_s_memrealtime();
  const bool hi8 = (lane & 8) != 0;
  const int b = pair >> 2, hkv = pair & 3;
  const f4 z = {0.f, 0.f, 0.f, 0.f};
  const v4u zu = {0, 0, 0, 0};
  if (wv == 2) {
    const int l = lane >> 5, f = lane & 31;
    const bool isO = f >= 24, is2 = (f >> 2) == 3, isK = f < 14, isB = s + (is2 ? 1 : 0) > 12;
    const u64 pk = isB ? (u64)p.kB : (u64)p.kA, pv = isB ? (u64)p.vB : (u64)p.vA, ptr = isO ? (u64)(br ? p.woB : p.woA) : isK ? pk : pv;
    const int o1 = (pair * SK + s * 640 - (s > 12 ? WIN : 0)) * 256, o2 = o1 + (512 - (s == 12 ? WIN : 0)) * 256;
    const int oO = (256 * (ri >> 2) + 32 * (ri & 3)) * (DM * 2) + (512 * ch + 256 * l) * 2;
    const int u = f + rot + (isK ? 4 : 0), rb = (4 * l + (rot >> 3)) * 2048;
    const int T = is2 ? 8 + ((f + (ILV == 2 ? pair : 0)) & 1) : (f + (isK ? 4 : 0) + (ILV == 2 ? pair * 5 : 0)) & 7;
    const int ck = s > 12 ? 96 + T * 3 + (s - 13) : !is2 ? T * 13 + s : s == 12 ? 118 + T : 8 + T * 12 + s;
    const int o = isO ? oO + ((u & 3) << 12) : (pair * 128 + ck) << 14;
    *(v4i*)(lds + L_Q + lane * 32) = v4i{o + 2048 + (isO ? 0 : rb & 0x3800), o + (isO ? 128 : (rb + 2048) & 0x3800), o + 2048 + (isO ? 256 : (rb + 4096) & 0x3800),
                                         o + (isO ? 384 : (rb + 6144) & 0x3800)};
    *(v4i*)(lds + L_Q + lane * 32 + 16) = v4i{(int)(unsigned)(ptr - 2048), (int)(unsigned)((ptr - 2048) >> 32) & 0xffff, f >= 28 ? 0 : (isO ? (int)WB : (int)KVB) + 2048,
                                              lds0 + (f >= 28 ? L_QR : l * RING + (f % NSLOT) * 8192)};
    const int vK = G * 256 + ((c16 ^ (2 * G)) * 16), vO = (16 * (lane >> 5) + 128 * (G & 1) + (lane & 8)) * (DM * 2) + (lane & 7) * 16;
    *(v4i*)(lds + L_Q + 2048 + lane * 16) = v4i{vK, vK ^ 128, vO, (vO ^ 32) + DM * 2 - 1024};
    asm volatile("s_waitcnt lgkmcnt(0)" ::: "memory");
  }
  const v4u xa = __builtin_amdgcn_raw_buffer_load_b128(mk(br ? p.xB : p.xA, 4 * DM * 2), ((c16 >> 2) * DM + 8 * (2 * G + (c16 & 1))) * 2,
                                                       (256 * kq + 64 * (w >> 1)) * 2, 0);
  PIN();
  __builtin_amdgcn_s_barrier();
  PIN();
  unsigned amp1 = (unsigned)(__builtin_amdgcn_s_memrealtime() - tc0);
  const rsrc_t rws = mk(p.ws, DBG == 1 ? 0 : WS_END);
  const u64 tag = DBG == 1 ? 0 : launch_tag(p.ws);
  const unsigned tl = (unsigned)(tag >> (16 * (lane & 3)));
  unsigned char* mine = lds + (w >> 2) * RING + (w & 3) * 2048;
  const int kr0 = (lane & 7) * 256 + (((hi8 ? 4 : 0) + G) ^ (2 * (lane & 7))) * 16;
  const int cdone = C_DONE + w;
#define RD(sl, o) (*(const v4u*)(mine + (sl) * 8192 + (o)))
#define TAKE(f, N, dst, T, SL_, O0, O1, SLP) do { await<SLP>(lds, C_FULL + (w >> 2), (8 * ((f) + (N) - 1) + 2 * (w & 3) + 2)); \
    _Pragma("unroll") for (int u_ = 0; u_ < (N); ++u_) { const int t_ = (T) + u_; dst[2 * t_] = RD(SL_, O0); dst[2 * t_ + 1] = RD(SL_, O1); } \
    if ((f) + (N) <= 28 - NSLOT + 1) { asm volatile("s_waitcnt lgkmcnt(0)" ::: "memory"); setw(lds, cdone, (f) + (N)); } \
    _Pragma("unroll") for (int u_ = 0; u_ < 2 * (N); ++u_) asm volatile("" : "+v"(dst[2 * (T) + u_])); } while (0)
#define TAKEK(f, N, dst, T) TAKE(f, N, dst, T, ((f) + u_) % NSLOT, kr0 ^ ((t_ & 1) * 64), kr0 ^ ((t_ & 1) * 64) ^ 128, KSL)
#define KEEP(x) asm volatile("" : "+v"(x))
#define UNTIL(t) do { _Pragma("unroll 1") while ((unsigned)(__builtin_amdgcn_s_memrealtime() - tc0) < (t)) __builtin_amdgcn_s_sleep(1); } while (0)
  v4u kB[4], kA[16], vt[8], wt[8];
  {
    TAKE(0, 4, vt, 0, (u_ - rot) & 3, lane * 16, lane * 16 + 1024, 1);
    tr64(vt, wt);
    f4 r = tile64(wt, xa, hi8);
    KEEP(r);
    *(f4*)(lds + L_QR + w * 1024 + lane * 16) = r;
    if ((unsigned)__builtin_amdgcn_readfirstlane(arrive(lds, C_QR, lane)) == (7u)) {
      const int un = 2 * (lane & 7) + (hi8 ? 1 : 0);
      const unsigned tu = (unsigned)(tag >> (16 * (un & 3)));
      const unsigned char* q = lds + L_QR + lane * 16;
      v4u q0 = mark((*(const f4*)(q) + *(const f4*)(q + 2048)) + (*(const f4*)(q + 4096) + *(const f4*)(q + 6144)), tu);
      v4u q1 = mark((*(const f4*)(q + 1024) + *(const f4*)(q + 3072)) + (*(const f4*)(q + 5120) + *(const f4*)(q + 7168)), tu);
      KEEP(q0); KEEP(q1);
      UNTIL(TQS);
      if (!(QX & 1)) {
        stc(q0, rws, G * 512 + un * 16, WS_QP + ((br * 32 + qh) * 4 + kq) * 2048);
        stc(q1, rws, G * 512 + un * 16, WS_QP + ((br * 32 + qh) * 4 + kq) * 2048 + 256);
      }
    }
  }
  STAMP(1);
  TAKEK(4, 2, kA, 0); TAKEK(6, 2, kA, 2);
  TAKEK(8, 2, kA, 4);
  const bool mixed = s == 12;
  {
    const int qo = WS_QP + ((s + (w >> 2) > 12 ? 32 : 0) + hkv * 8 + 2 * (w & 3)) * 8192 + b * 512, qv = (lane >> 5) * 8192 + (lane & 31) * 16;
    const bool doq = w < 4 || mixed;
    v4u g[4] = {zu, zu, zu, zu};
    if (doq && !(QX & 2)) {
#pragma unroll
      for (int u = 0; u < 4; ++u) g[u] = ldc(rws, qv, qo + u * 2048);
    }
    PIN();
    TAKEK(10, 2, kA, 6);
    if (doq) {
      if (!QX) settle<4, S1>(g, tl, lane, rws, qv, qo, 2048);
      const f4 q = (val(g[0]) + val(g[1])) + (val(g[2]) + val(g[3]));
      v2u qb = {pk2(q[0], q[1]), pk2(q[2], q[3])};
      KEEP(qb);
      *(v2u*)(lds + L_Q + (2 * w + (lane >> 5)) * QSTR + (lane & 31) * 8) = qb;
    }
    arrive(lds, C_Q, lane);
  }
  STAMP(2);
  TAKEK(12, 2, kB, 0);
  await<1>(lds, C_Q, 8);
  STAMP(3);
  constexpr float C2 = 1.4426950408889634f;
  float inv, lse;
  {
    const unsigned char* qp = lds + L_Q + (lane & 7) * QSTR + (hi8 ? 64 : 0) + G * 16;
    const bool t0lane = (G >> 1) == (c16 >> 3);
    const v4u qa0 = *(const v4u*)qp, qa1 = *(const v4u*)(qp + 128), qb0 = *(const v4u*)(qp + (mixed ? 8 * QSTR : 0)), qb1 = *(const v4u*)(qp + (mixed ? 8 * QSTR : 0) + 128);
    f4 d[10];
#pragma unroll
    for (int u = 0; u < 8; ++u) d[u] = mfma32(kA[2 * u], qa0, z);
    d[8] = mfma32(kB[0], qb0, z); d[9] = mfma32(kB[2], qb0, z);
#pragma unroll
    for (int u = 0; u < 8; ++u) d[u] = mfma32(kA[2 * u + 1], qa1, d[u]);
    d[8] = mfma32(kB[1], qb1, d[8]); d[9] = mfma32(kB[3], qb1, d[9]);
    PIN();
    float za[8], zb[2];
#pragma unroll
    for (int u = 0; u < 4; ++u) qk_fin(d[2 * u], d[2 * u + 1], t0lane, za[2 * u], za[2 * u + 1]);
    qk_fin(d[8], d[9], t0lane, zb[0], zb[1]);
    float mA = fmaxf(fmaxf(fmaxf(za[0], za[1]), fmaxf(za[2], za[3])), fmaxf(fmaxf(za[4], za[5]), fmaxf(za[6], za[7])));
    mA = max_x8_16_32(mA) * C2;
    const float mB = max_x8_16_32(fmaxf(zb[0], zb[1])) * C2;
    float lA = 0.f;
#pragma unroll
    for (int u = 0; u < 8; ++u) { za[u] = __builtin_amdgcn_exp2f(fmaf(za[u], C2, -mA)); lA += za[u]; }
    zb[0] = __builtin_amdgcn_exp2f(fmaf(zb[0], C2, -mB)); zb[1] = __builtin_amdgcn_exp2f(fmaf(zb[1], C2, -mB));
    lA = sum_x8_16_32(lA);
    const float lB = sum_x8_16_32(zb[0] + zb[1]);
    if (lane < 16) *(float2*)(lds + L_ML + (lane * 8 + w) * 8) = hi8 ? make_float2(mB, lB) : make_float2(mA, lA);
    arrive(lds, C_ML, lane);
    await<1>(lds, C_ML, 8);
    f4 mv[4];
#pragma unroll
    for (int u = 0; u < 4; ++u) mv[u] = *(const f4*)(lds + L_ML + c16 * 64 + u * 16);
    float Mw = fmaxf(fmaxf(fmaxf(mv[0][0], mv[0][2]), fmaxf(mv[1][0], mv[1][2])), fmaxf(fmaxf(mv[2][0], mv[2][2]), fmaxf(mv[3][0], mv[3][2])));
    float Mo = ror8(Mw);
    if (!mixed) Mo = Mw = fmaxf(Mw, Mo);
    float Lw = 0.f;
#pragma unroll
    for (int u = 0; u < 4; ++u) { Lw = fmaf(__builtin_amdgcn_exp2f(mv[u][0] - Mw), mv[u][1], Lw); Lw = fmaf(__builtin_amdgcn_exp2f(mv[u][2] - Mw), mv[u][3], Lw); }
    const float Lo = ror8(Lw);
    if (!mixed) Lw += Lo;
    inv = 1.0f / Lw;
    lse = Mw + __builtin_amdgcn_logf(Lw);
    const float fA = __builtin_amdgcn_exp2f(mA - (hi8 ? Mo : Mw)), fB = __builtin_amdgcn_exp2f(mB - (hi8 ? Mw : Mo));
    unsigned char* pw = lds + L_QR + (hi8 ? 1024 : 0) + (lane & 7) * 128 + ((w ^ (lane & 6)) * 16) + ((G ^ (2 * (w & 1))) * 4);
    {
#pragma unroll
      for (int u = 0; u < 4; ++u) *(unsigned*)(pw + u * 2048) = pk2(za[2 * u] * fA, za[2 * u + 1] * fA);
      *(unsigned*)(pw + 4 * 2048) = pk2(zb[0] * fB, zb[1] * fB);
    }
    arrive(lds, C_P, lane);
  }
  STAMP(4);
  v4u pf[20];
  await<1>(lds, C_P, 8);
  {
    const unsigned char* pr = lds + L_QR + (lane & 7) * 128;
#pragma unroll
    for (int t = 0; t < 10; ++t) { pf[2 * t] = *(const v4u*)(pr + t * 1024 + ((G ^ (lane & 6)) * 16)); pf[2 * t + 1] = *(const v4u*)(pr + t * 1024 + (((4 + G) ^ (lane & 6)) * 16)); }
#pragma unroll
    for (int t = 0; t < 20; ++t) KEEP(pf[t]);
  }
  const int row0 = 2 * (c16 >> 2) + (G & 1), row1 = row0 ^ 1, chk = 2 * w + ((lane >> 1) & 1);
  const unsigned char* t0 = lds + G * 2048 + row0 * 256 + ((chk ^ (2 * row0)) * 16) + 8 * (lane & 1);
  const unsigned char* t1 = lds + G * 2048 + row1 * 256 + ((chk ^ (2 * row1)) * 16) + 8 * (lane & 1);
#define AFRAG(f, l) ({ const v2u x_ = trd(t0 + (l) * RING + ((f) % NSLOT) * 8192), y_ = trd(t1 + (l) * RING + ((f) % NSLOT) * 8192); v4u{x_[0], x_[1], y_[0], y_[1]}; })
#define PV(f, N, T, acc, SLP) do { await_both<SLP>(lds, (8 * ((f) + (N)))); v4u a_[2 * (N)]; \
    _Pragma("unroll") for (int u_ = 0; u_ < (N); ++u_) { a_[2 * u_] = AFRAG((f) + u_, 0); a_[2 * u_ + 1] = AFRAG((f) + u_, 1); } \
    if ((f) < 28 - NSLOT) { asm volatile("s_waitcnt lgkmcnt(0)" ::: "memory"); setw(lds, cdone, (f) + (N)); } \
    _Pragma("unroll") for (int u_ = 0; u_ < 2 * (N); ++u_) acc = mfma32(a_[u_], pf[2 * (T) + u_], acc); KEEP(acc); } while (0)
  f4 o2 = z;
  PV(14, 2, 8, o2, 1);
  f4 o1 = mixed ? z : o2;
  PV(16, 2, 0, o1, 1);
  if (s == 5 && w == 0) {
    const unsigned tb = (unsigned)__builtin_amdgcn_s_memrealtime(), te = tb + (tb - (unsigned)tc0) * ADP / 1024u + ADP0;
    stc(v4u{te, (unsigned)tag, te, (unsigned)(tag >> 32)}, rws, lane == 0 ? pair * 128 : OOB, WS_PAD);
  }
  PV(18, 2, 2, o1, 1);
  STAMP(5);
  const unsigned tg = (unsigned)(tag >> (16 * G)), xf = (G & 1) ? fu(lse) >> 16 : fu(lse);
  unsigned me = (tg & 0x0f0fu) | ((xf & 0x0f0fu) << 4), mo = ((tg >> 4) & 0x0f0fu) | (xf & 0xf0f0u);
  int sa = hi8 && !mixed ? OOB : (hi8 ? 4096 : 0) + (lane & 7) * 64 + G * 16;
  const bool e0 = lane < (br ? 32 : 64), e1 = lane < (br ? 0 : 40);
  int ovv[2] = {e0 ? (lane >> 3) * 4096 + (lane & 7) * 16 : OOB, e1 ? (8 + (lane >> 3)) * 4096 + (lane & 7) * 16 : OOB};
  unsigned tt[2] = {e0 ? tl : 0u, e1 ? tl : 0u}, xs = (lane & 1) ? 0x05040100u : 0x01000504u;
  float n0 = e0 ? 0.f : -INFINITY, n1 = e1 ? 0.f : -INFINITY;
  KEEP(xs); KEEP(n0); KEEP(n1);
  int oa = lds0 + L_O + ((w >> 1) * 8 + 4 * (w & 1) + ((lane & 7) >> 1)) * 16 + ((2 * (lane & 1) + (lane >> 5) - rot) & 3) * 4 + (G & 1) * 2;
  int ra = lds0 + L_O + ((c16 >> 2) * 8 + G) * 16, one = 1;
  int ca = lds0 + L_CTL + C_O * 4;
  int wa[8];
#pragma unroll
  for (int u = 0; u < 8; ++u) {
    const int q = c16 >> 2, sl = 6 + 2 * (u >> 2) + (q >> 1);
    wa[u] = (w >> 2) * RING + (w & 3) * 2048 + (sl == NSLOT ? 0 : sl) * 8192 + (q & 1) * 1024 + G * 128 + (q ^ (u & 3)) * 32 + (lane & 3) * 8;
    KEEP(wa[u]);
  }
  int pv = G * (16 * 8192) + c16 * 16;
  int fr = lds0 + L_Q + lane * 32, fa = fr + w * 4;
  int so = WS_OP + (pair * 17 + s + (s > 12)) * 4096 + w * 512, sp = WS_P2 + (br * 128 + 2 * ri + ch) * 8192 + w * 1024;
  int og = WS_OP + (((w >> 1) * 4 + (ri >> 4)) * 17 + br * 13) * 4096 + (2 * (ri & 3) + (w & 1)) * 512 + ((ri >> 2) & 3) * 128;
  int pg = WS_P2 + (br * 128 + 2 * w + ch) * 8192 + (ri & 31) * 256, xo = ((ri & 3) * 2048 + br * 1024 + 512 * ch + 64 * (ri >> 2)) * 2;
  KEEP(inv); KEEP(me); KEEP(mo); KEEP(sa); KEEP(ovv[0]); KEEP(ovv[1]); KEEP(tt[0]); KEEP(tt[1]); KEEP(oa); KEEP(ra); KEEP(one); KEEP(fr); KEEP(ca); KEEP(pv); KEEP(fa);
  asm volatile("" : "+s"(so), "+s"(sp), "+s"(og), "+s"(pg), "+s"(xo));
  unsigned cpf = 0;
  if (w == 1 && (s & 3) == 0) {
    u64 pc;
    asm volatile("s_getpc_b64 %0" : "=s"(pc));
    cpf = __builtin_amdgcn_raw_buffer_load_b32(mk((const void*)pc, CPF), lane * 64, 0, 0);
  }
  PIN();
  PV(20, 2, 4, o1, 1);
  v4u bc = zu;
  if (w == 0) bc = ldc(rws, lane < 16 ? lane * 128 : OOB, WS_PAD);
  PIN();
  PV(22, 1, 6, o1, 1);
  unsigned amp2 = (unsigned)(__builtin_amdgcn_s_memrealtime() - tc0);
  if (w == 0) {
    bool ok;
#pragma unroll 1
    for (;;) {
      ok = bc[1] == (unsigned)tag && bc[3] == (unsigned)(tag >> 32);
      if (!wave_bad(lane < 16 && !ok) || (unsigned)(__builtin_amdgcn_s_memrealtime() - tc0) > 2000u) break;
      __builtin_amdgcn_s_sleep(8);
      CB();
      const v4u nb = ldc(rws, lane < 16 && !ok ? lane * 128 : OOB, WS_PAD);
      if (!ok) bc = nb;
    }
    float dt = ok ? (float)(int)(bc[0] - (unsigned)tc0) : 0.f, dm = dt;
    dt += __shfl_xor(dt, 1); dt += __shfl_xor(dt, 2); dt += __shfl_xor(dt, 4); dt += ror8(dt);
    dm = fmaxf(dm, __shfl_xor(dm, 1)); dm = fmaxf(dm, __shfl_xor(dm, 2)); dm = fmaxf(dm, __shfl_xor(dm, 4)); dm = fmaxf(dm, ror8(dm));
    const int rel = min(max(__builtin_amdgcn_readfirstlane((int)(dt * ((100 - AMX) / 1600.f) + dm * (AMX / 100.f))), 1200), 2100);
    setw(lds, C_TG + 4, (unsigned)tc0 + rel);
    setw(lds, C_TG, 1);
  }
  await<1>(lds, C_TG, 1);
  const unsigned tga = __builtin_amdgcn_readfirstlane(__hip_atomic_load((lds_u32*)(lds + L_CTL) + C_TG + 4, __ATOMIC_RELAXED, __HIP_MEMORY_SCOPE_WORKGROUP));
  const unsigned tgd = max(min(tga - (unsigned)tc0, 2200u), 1000u);
  KEEP(cpf);
  UNTIL(tgd - TPD);
  PV(23, 1, 7, o1, 0);
  STAMP(6);
  stc(mark8(f4{hi8 ? o2[0] : o1[0], hi8 ? o2[1] : o1[1], hi8 ? o2[2] : o1[2], hi8 ? o2[3] : o1[3]} * inv, me, mo), rws, sa, so);
  unsigned pre1 = 0;
  await<0>(lds, C_ISS, GI); await<0>(lds, C_ISS + 1, GI);
  pre1 = __builtin_amdgcn_raw_buffer_load_b32(rws, lane < 8 ? lane * 128 : OOB, sp, AUX_SC1);
  PIN();
  STAMP(7);
  {
    v4u g[2];
    UNTIL(tgd);
    g[0] = ldc(rws, ovv[0], og); g[1] = ldc(rws, ovv[1], og);
    STAMP(8);
    PIN();
    await<0>(lds, C_FULL + (w >> 2), 8 * 27 + 2 * (w & 3) + 2);
#pragma unroll
    for (int u = 0; u < 8; ++u) {
      const v2u x = trd(lds + wa[u & 3] + (u >> 2) * 512), yv = trd(lds + wa[4 + (u & 3)] + (u >> 2) * 512);
      wt[u] = v4u{x[0], x[1], yv[0], yv[1]};
    }
#pragma unroll
    for (int u = 0; u < 8; ++u) KEEP(wt[u]);
    PIN();
    settle2<S4>(g, tt, lane, rws, ovv, og);
    KEEP(pre1);
    STAMP(9);
    const float x0 = lse8(g[0], xs) + n0, x1 = lse8(g[1], xs) + n1;
    const float M = max_x8_16_32(mx(x0, x1));
    const float w0 = __builtin_amdgcn_exp2f(x0 - M), w1 = __builtin_amdgcn_exp2f(x1 - M);
    const float y = fold4(val8(g[0]) * w0 + val8(g[1]) * w1) * __builtin_amdgcn_rcpf(sum_x8_16_32(w0 + w1));
    asm volatile("ds_write_b16 %0, %1\n\ts_mov_b64 exec, 1\n\tds_add_u32 %2, %3\n\ts_mov_b64 exec, -1" :: "v"(oa), "v"(pk2(y, y)), "v"(ca), "v"(one) : "memory");
    v4u a0, a1;
#pragma unroll 1
    for (unsigned spins = 0; spins < LSPIN_CAP; ++spins) {
      int c;
      asm volatile("ds_read_b32 %0, %3\n\tds_read_b128 %1, %4\n\tds_read_b128 %2, %4 offset:64\n\ts_waitcnt lgkmcnt(0)" : "=&v"(c), "=&v"(a0), "=&v"(a1) : "v"(ca), "v"(ra) : "memory");
      if (__builtin_amdgcn_readfirstlane(c) >= 8) break;
    }
    f4 d[4];
#pragma unroll
    for (int n = 0; n < 4; ++n) d[n] = mfma32(a0, wt[n], z);
    PIN();
#pragma unroll
    for (int n = 0; n < 4; ++n) d[n] = mfma32(a1, wt[4 + n], d[n]);
    PIN();
    stc(mark(f4{d[0][0], d[1][0], d[2][0], d[3][0]}, tl), rws, lane * 16, sp);
  }
  STAMP(11);
  if (ri < 32) {
    __builtin_amdgcn_s_sleep(D2);
    v4u g[2] = {ldc(rws, pv, pg), ldc(rws, pv, pg + 64 * 8192)};
    STAMP(12);
    settle<2, S5>(g, tl, lane, rws, pv, pg, 64 * 8192);
    STAMP(14);
    const f4 v = val(g[0]) + val(g[1]);
    int old;
    f4 u0, u1;
    asm volatile("ds_write_b32 %3, %4\n\ts_mov_b64 exec, 1\n\tds_add_rtn_u32 %0, %5, %6 offset:%8\n\ts_mov_b64 exec, -1\n\t"
                 "ds_read_b128 %1, %7\n\tds_read_b128 %2, %7 offset:16\n\ts_waitcnt lgkmcnt(0)"
                 : "=&v"(old), "=&v"(u0), "=&v"(u1) : "v"(fa), "v"(red16(red32(v[0], v[2]), red32(v[1], v[3]))), "v"(ca), "v"(one), "v"(fr), "i"((C_F - C_O) * 4) : "memory");
    if (__builtin_amdgcn_readfirstlane(old) == 7) {
      const float r = ((u0[0] + u0[1]) + (u0[2] + u0[3])) + ((u1[0] + u1[1]) + (u1[2] + u1[3]));
      __builtin_amdgcn_raw_buffer_store_b16((short)pk2(r, r), mk(p.out, 4 * 2048 * 2), lane * 2, xo, OAUX);
      if (j == 0 && !EPE) __builtin_amdgcn_raw_buffer_store_b64(v2u{(unsigned)tag, (unsigned)(tag >> 32)}, rws, lane == 0 ? 0 : OOB, WS_EPOCH, AUX_SC1);
    }
  }
  if (w == 1 && (AMPL == 2 || j == 0)) {
    const unsigned amp0 = (unsigned)(__builtin_amdgcn_s_memrealtime() - tc0);
    const unsigned t = 4000u + 10u * (AMPL == 0 ? amp0 - 1500u : AMPL == 1 ? amp1 : AMPL == 2 ? amp2 - 1200u : 0u);
    UNTIL(min(t, 20000u));
  }
  STAMP(10);
  STAMP_OUT();
  return;
  }

  {
    __builtin_amdgcn_s_setprio(3);
    const int l = wv;
    const int full_addr = lds0 + L_CTL + (C_FULL + l) * 4, done_addr = lds0 + L_CTL + C_DONE * 4;
    const int ring0 = lds0 + l * RING;
    publish(lds0 + L_CTL + ((lane & 31) == C_FULL + 1 - l ? 0 : (lane & 31)) * 4, 0);
    int v0 = (8 * (lane >> 3)) * (NQ * 2) + (lane & 7) * 16, v1 = v0 + NQ * 2 - 1024;
    v4i vp = {v0, v1, 0, 0};
    const u64 tl0 = __builtin_amdgcn_s_memrealtime();
    int wide = 0, cnt = -2 - (WND & ~1), freed = 2;
    const int q0 = (256 * kq + 128 * l + 1) * (NQ * 2) + qh * 256 + (((lane + rot) & 3) << 14);
    v4i ta = {q0, q0 + 128 - 2048, q0 + 64 * NQ * 2, q0 + 64 * NQ * 2 + 128 - 2048}, tb = srd((const char*)(br ? p.wqB : p.wqA) - NQ * 2, WB + NQ * 2);
    tb[3] = ring0 + lane * 8192;
#pragma unroll 1
    for (int f = 0; f < 31; ++f) {
      v4i r = {__builtin_amdgcn_readlane(tb[0], f), __builtin_amdgcn_readlane(tb[1], f), __builtin_amdgcn_readlane(tb[2], f), 0x00020000};
      int o0 = __builtin_amdgcn_readlane(ta[0], f), o1 = __builtin_amdgcn_readlane(ta[1], f);
      const int dst = __builtin_amdgcn_readlane(tb[3], f), o2 = __builtin_amdgcn_readlane(ta[2], f), o3 = __builtin_amdgcn_readlane(ta[3], f);
      asm volatile("s_nop 3" : "+s"(r), "+s"(o0), "+s"(o1));
      dma4<WND, WHI>(dst, v0, v1, r, o0, o1, full_addr, cnt, wide);
      dma4<WND, WHI>(dst + 4096, v0, v1, r, o2, o3, full_addr, cnt, wide);
      const int go = ((f == FW - 1) & ((unsigned)(__builtin_amdgcn_s_memrealtime() - tl0) >= TL)) ? WHI - WND : 0;
      wide |= go;
      cnt -= go;
      publish(full_addr + (C_ISS - C_FULL) * 4, f);
      asm volatile("s_cmp_lg_u32 %3, 3\n\ts_cbranch_scc1 1f\n\ts_waitcnt lgkmcnt(0)\n\ts_barrier\n\t"
                   "ds_read_b128 %0, %4\n\tds_read_b128 %1, %4 offset:16\n\tds_read_b128 %2, %5\n\ts_waitcnt lgkmcnt(0)\n1:"
                   : "+v"(ta), "+v"(tb), "+v"(vp) : "s"(f), "v"(lds0 + L_Q + l * 1024 + (lane & 31) * 32), "v"(lds0 + L_Q + 2048 + lane * 16) : "scc", "memory");
      v0 = f < 23 ? vp[0] : vp[2] ^ (~f & 1) << 6;
      v1 = f < 23 ? vp[1] : vp[3] ^ (~f & 1) << 6;
      if (f + 2 >= freed) {
#pragma unroll 1
        for (unsigned spins = 0; spins < LSPIN_CAP; ++spins) {
          v4i d, e;
          asm volatile("ds_read_b128 %0, %2\n\tds_read_b128 %1, %2 offset:16\n\ts_waitcnt lgkmcnt(0)" : "=&v"(d), "=&v"(e) : "v"(done_addr) : "memory");
          freed = (DBG == 5 ? 1000 : NSLOT) + __builtin_amdgcn_readfirstlane(min(min(min(d[0], d[1]), min(d[2], d[3])), min(min(e[0], e[1]), min(e[2], e[3]))));
          if (freed >= 28) freed = 64;
          if (__builtin_expect(f + 1 < freed, 1)) break;
          asm volatile("s_waitcnt vmcnt(0)" ::: "memory");
          publish(full_addr, 8 * f + 8);
          __builtin_amdgcn_s_sleep(1);
        }
      }
    }
    asm volatile("s_waitcnt vmcnt(0)" ::: "memory");
    STAMP(5);
    STAMP_OUT();
  }

}

extern "C" void kernel_launch(void* const* d_in, const int* in_sizes, int n_in, void* d_out, int out_size, void* d_ws, size_t ws_size,
                              hipStream_t stream) {
  Params p;
  p.xA = d_in[0]; p.kA = d_in[1]; p.vA = d_in[2]; p.wqA = d_in[3]; p.woA = d_in[4];
  p.xB = d_in[5]; p.kB = d_in[6]; p.vB = d_in[7]; p.wqB = d_in[8]; p.woB = d_in[9];
  p.out = d_out; p.ws = d_ws;
  fused_decode<<<NWG, NTHR, 0, stream>>>(p);
}
```

```cpp
#include <hip/hip_runtime.h>
#include <hip/hip_bf16.h>
#include <stdint.h>

typedef unsigned v4u __attribute__((ext_vector_type(4)));
typedef unsigned v2u __attribute__((ext_vector_type(2)));
typedef int v4i __attribute__((ext_vector_type(4)));
typedef float f4 __attribute__((ext_vector_type(4)));
typedef __bf16 bf8 __attribute__((ext_vector_type(8)));
typedef short s4 __attribute__((ext_vector_type(4)));
typedef __amdgpu_buffer_rsrc_t rsrc_t;
typedef unsigned long long u64;
typedef __attribute__((address_space(3))) unsigned lds_u32;

constexpr int WND = 10, WHI = 22;
constexpr unsigned TL = 1400;
constexpr unsigned TQS = 470;
constexpr unsigned ADP = 425, AMX = 60;
constexpr unsigned TPD = 75;
constexpr int D2 = 15;
constexpr int CPF = 3260;
static_assert(WHI <= 22 && (WHI - WND) % 2 == 0, "three empty fills drain the window; both windows publish behind the same requests");

constexpr int NWG = 256, NTHR = 640;
constexpr int SK = 8192, DH = 128, DM = 1024, NQ = 4096;
constexpr unsigned WS_EPOCH = 0;
constexpr unsigned WS_QP = 4096;
constexpr unsigned WS_OP = WS_QP + 2 * 4 * 4 * 4096 * 4;
constexpr unsigned WS_PAD = WS_OP + 2 * 4 * 32 * 16 * 512;
constexpr unsigned WS_P2 = WS_PAD + 8192;
constexpr unsigned WS_END = WS_P2 + 2 * 2 * 64 * 64 * 128;
constexpr int AUX_SC1 = 16, OOB = 0x7ffffff0;
constexpr unsigned SPIN_CAP = 1u << 21, LSPIN_CAP = 1u << 23;
constexpr int NSLOT = 9;
constexpr int RING = NSLOT * 8192;
constexpr int L_QR = 2 * RING;
constexpr int L_Q = L_QR + 10240;
constexpr int QSTR = 272;
constexpr int L_ML = L_Q + 2 * 8 * QSTR;
constexpr int L_O = L_ML + 2 * 8 * 8 * 8;
constexpr int L_CTL = L_O + 512;
constexpr int L_END = L_CTL + 128;
enum { C_DONE = 0, C_FULL = 8, C_QR = 10, C_Q = 11, C_ML = 12, C_P = 13, C_O = 14, C_TG = 16  , C_F = 17, C_ISS = 18   };

struct Params {
  const void *wqA, *wqB, *kA, *kB, *vB, *vA, *woA, *woB, *xA, *xB;
  void *out, *ws;
};

#define PIN() __builtin_amdgcn_sched_barrier(0)
#define CB() asm volatile("" ::: "memory")
__device__ __forceinline__ rsrc_t mk(const void* p, unsigned bytes) { return __builtin_amdgcn_make_buffer_rsrc((void*)p, 0, (int)bytes, 0x00020000); }
__device__ __forceinline__ v4u ldc(rsrc_t r, int voff, int soff) { return __builtin_amdgcn_raw_buffer_load_b128(r, voff, soff, AUX_SC1); }
__device__ __forceinline__ void stc(v4u v, rsrc_t r, int voff, int soff) {
  __builtin_amdgcn_raw_buffer_store_b128(v, r, voff, soff, AUX_SC1);
  asm volatile("s_nop 1" :: "v"(v) : "memory");
}
__device__ __forceinline__ f4 mfma32(v4u a, v4u b, f4 c) { return __builtin_amdgcn_mfma_f32_16x16x32_bf16(__builtin_bit_cast(bf8, a), __builtin_bit_cast(bf8, b), c, 0, 0, 0); }
__device__ __forceinline__ unsigned fu(float x) { return __builtin_bit_cast(unsigned, x); }
__device__ __forceinline__ float uf(unsigned x) { return __builtin_bit_cast(float, x); }
__device__ __forceinline__ unsigned ror8u(unsigned x) { return (unsigned)__builtin_amdgcn_mov_dpp((int)x, 0x128, 0xf, 0xf, true); }
__device__ __forceinline__ float ror8(float x) { return uf(ror8u(fu(x))); }
template <int C> __device__ __forceinline__ float dpp(float x) { return uf((unsigned)__builtin_amdgcn_mov_dpp((int)fu(x), C, 0xf, 0xf, true)); }
__device__ __forceinline__ unsigned pk2(float lo, float hi) {
  typedef __bf16 bf2 __attribute__((ext_vector_type(2)));
  typedef float f2 __attribute__((ext_vector_type(2)));
  f2 v = {lo, hi};
  return __builtin_bit_cast(unsigned, __builtin_convertvector(v, bf2));
}
template <int T> __device__ __forceinline__ unsigned tr(const v4u& a, const v4u& b) {
  return __builtin_amdgcn_perm(b[T >> 1], a[T >> 1], (T & 1) ? 0x07060302u : 0x05040100u);
}
template <int T> __device__ __forceinline__ v4u tr8(const v4u* r) { return v4u{tr<T>(r[0], r[1]), tr<T>(r[2], r[3]), tr<T>(r[4], r[5]), tr<T>(r[6], r[7])}; }
__device__ __forceinline__ void tr64(const v4u* wr, v4u* wt) {
  wt[0] = tr8<0>(wr); wt[1] = tr8<1>(wr); wt[2] = tr8<2>(wr); wt[3] = tr8<3>(wr);
  wt[4] = tr8<4>(wr); wt[5] = tr8<5>(wr); wt[6] = tr8<6>(wr); wt[7] = tr8<7>(wr);
}
__device__ __forceinline__ float red32(float a, float b) { auto s = __builtin_amdgcn_permlane32_swap(fu(a), fu(b), false, false); return uf(s[0]) + uf(s[1]); }
__device__ __forceinline__ float red16(float a, float b) { auto s = __builtin_amdgcn_permlane16_swap(fu(a), fu(b), false, false); return uf(s[0]) + uf(s[1]); }
__device__ __forceinline__ float mx(float a, float b) { float r; asm("v_max_f32 %0, %1, %2" : "=v"(r) : "v"(a), "v"(b)); return r; }
__device__ __forceinline__ float max_x8_16_32(float x) {
  x = mx(x, ror8(x));
  auto s = __builtin_amdgcn_permlane16_swap(fu(x), fu(x), false, false); x = mx(uf(s[0]), uf(s[1]));
  auto t = __builtin_amdgcn_permlane32_swap(fu(x), fu(x), false, false); return mx(uf(t[0]), uf(t[1]));
}
__device__ __forceinline__ float sum_x8_16_32(float x) { x = x + ror8(x); x = red16(x, x); return red32(x, x); }
__device__ __forceinline__ float fold4(f4 v) {
  const float y = red16(red32(v[0], v[2]), red32(v[1], v[3]));
  return y + ror8(y);
}
__device__ __forceinline__ float sel(bool c, float a, float b) { asm volatile("" : "+v"(a), "+v"(b)); return c ? a : b; }
__device__ __forceinline__ f4 tile64(const v4u* wt, v4u a, bool hi8) {
  const f4 z = {0.f, 0.f, 0.f, 0.f};
  f4 d[8], r;
#pragma unroll
  for (int t = 0; t < 4; ++t) { d[t] = mfma32(a, wt[t], z); d[t + 4] = mfma32(a, wt[t + 4], z); }
#pragma unroll
  for (int u = 0; u < 4; ++u) r[u] = sel(hi8, d[4 + u][1], d[u][0]) + ror8(sel(hi8, d[u][1], d[4 + u][0]));
  return r;
}
__device__ __forceinline__ void qk_fin(const f4& da, const f4& db, bool t0lane, float& z0, float& z1) {
  const float x0 = t0lane ? da[0] : db[0], x1 = t0lane ? da[1] : db[1], x2 = t0lane ? da[2] : db[2], x3 = t0lane ? da[3] : db[3];
  auto s = __builtin_amdgcn_permlane32_swap(fu(x0), fu(x1), false, false);
  auto t = __builtin_amdgcn_permlane32_swap(fu(x2), fu(x3), false, false);
  const unsigned r1 = pk2(uf(s[0]) + ror8(uf(s[1])), uf(t[0]) + ror8(uf(t[1])));
  const unsigned r2 = pk2(uf(r1 << 16) * 0.088388347648318440f, uf(r1 & 0xffff0000u) * 0.088388347648318440f);
  z0 = uf(r2 << 16);
  z1 = uf(r2 & 0xffff0000u);
}
__device__ __forceinline__ v4u mark(f4 v, unsigned tf) {
  const v4u u = __builtin_bit_cast(v4u, v);
  return v4u{(u[0] & ~15u) | (tf & 15u), (u[1] & ~15u) | ((tf >> 4) & 15u), (u[2] & ~15u) | ((tf >> 8) & 15u), (u[3] & ~15u) | ((tf >> 12) & 15u)};
}
__device__ __forceinline__ unsigned mis(const v4u& g, unsigned tf) { return ((g[0] ^ tf) | (g[1] ^ (tf >> 4)) | (g[2] ^ (tf >> 8)) | (g[3] ^ (tf >> 12))) & 15u; }
__device__ __forceinline__ f4 val(const v4u& g) { return __builtin_bit_cast(f4, g & ~15u); }
__device__ __forceinline__ v4u mark8(f4 v, unsigned e, unsigned o) {
  const v4u u = __builtin_bit_cast(v4u, v);
  return v4u{(u[0] & ~255u) | (e & 255u), (u[1] & ~255u) | (o & 255u), (u[2] & ~255u) | (e >> 8), (u[3] & ~255u) | (o >> 8)};
}
__device__ __forceinline__ f4 val8(const v4u& g) { return __builtin_bit_cast(f4, g & ~255u); }
__device__ __forceinline__ unsigned side16(const v4u& g) {
  const unsigned e = __builtin_amdgcn_perm(g[2], g[0], 0x0c0c0400u), o = __builtin_amdgcn_perm(g[3], g[1], 0x0c0c0400u);
  return ((e >> 4) & 0x0f0fu) | (o & 0xf0f0u);
}
__device__ __forceinline__ float lse8(const v4u& g, unsigned xs) {
  const unsigned x = side16(g);
  const unsigned y = (unsigned)__builtin_amdgcn_mov_dpp((int)x, 0xB1, 0xf, 0xf, true);
  return uf(__builtin_amdgcn_perm(x, y, xs));
}
__device__ __forceinline__ bool wave_bad(bool bad) { return __builtin_amdgcn_ballot_w64(bad) != 0ull; }
template <int N, int SLEEP> __device__ __forceinline__ unsigned settle(v4u* g, unsigned tf, int lane, rsrc_t r, int voff, int soff, int stride) {
  unsigned spins = 0;
  soff = __builtin_amdgcn_readfirstlane(soff);
#pragma unroll 1
  for (; spins < SPIN_CAP; ++spins) {
    unsigned m[N], any = 0;
#pragma unroll
    for (int u = 0; u < N; ++u) { m[u] = mis(g[u], tf); any |= m[u]; }
    if (__builtin_expect(!wave_bad(any != 0u), 1)) break;
    if (SLEEP) __builtin_amdgcn_s_sleep(SLEEP);
    CB();
    bool bad[N];
    v4u n[N];
#pragma unroll
    for (int u = 0; u < N; ++u) {
      bad[u] = ((__builtin_amdgcn_ballot_w64(m[u] != 0u) >> (lane & 56)) & 0xffull) != 0ull;
      n[u] = ldc(r, bad[u] ? voff : OOB, soff + u * stride);
    }
    PIN();
#pragma unroll
    for (int u = 0; u < N; ++u) g[u] = v4u{bad[u] ? n[u][0] : g[u][0], bad[u] ? n[u][1] : g[u][1], bad[u] ? n[u][2] : g[u][2], bad[u] ? n[u][3] : g[u][3]};
  }
  return spins;
}
template <int SLEEP> __device__ __forceinline__ void settle2(v4u* g, const unsigned* tf, int lane, rsrc_t r, const int* voff, int soff) {
  soff = __builtin_amdgcn_readfirstlane(soff);
#pragma unroll 1
  for (unsigned spins = 0; spins < SPIN_CAP; ++spins) {
    const unsigned m[2] = {mis(g[0], tf[0]), mis(g[1], tf[1])};
    if (__builtin_expect(!wave_bad((m[0] | m[1]) != 0u), 1)) break;
    if (SLEEP) __builtin_amdgcn_s_sleep(SLEEP);
    CB();
    bool bad[2];
    v4u n[2];
#pragma unroll
    for (int u = 0; u < 2; ++u) {
      bad[u] = ((__builtin_amdgcn_ballot_w64(m[u] != 0u) >> (lane & 56)) & 0xffull) != 0ull;
      n[u] = ldc(r, bad[u] ? voff[u] : OOB, soff);
    }
    PIN();
#pragma unroll
    for (int u = 0; u < 2; ++u) g[u] = v4u{bad[u] ? n[u][0] : g[u][0], bad[u] ? n[u][1] : g[u][1], bad[u] ? n[u][2] : g[u][2], bad[u] ? n[u][3] : g[u][3]};
  }
}
__device__ __forceinline__ u64 launch_tag(const void* ws) {
  u64 e;
  asm volatile("s_load_dwordx2 %0, %1, 0x0\n\ts_waitcnt lgkmcnt(0)" : "=s"(e) : "s"(ws) : "memory");
  u64 d = (e + 0x9E3779B97F4A7C15ull) * 0xBF58476D1CE4E5B9ull;
  d ^= d >> 29;
  return e ^ (d | 0x1111111111111111ull);
}
__device__ __forceinline__ unsigned arrive(unsigned char* lds, int c, int lane) {
  unsigned old = 0;
  CB();
  if (lane == 0) old = __hip_atomic_fetch_add((lds_u32*)(lds + L_CTL) + c, 1u, __ATOMIC_RELAXED, __HIP_MEMORY_SCOPE_WORKGROUP);
  CB();
  return old;
}
__device__ __forceinline__ void setw(unsigned char* lds, int c, unsigned v) {
  CB();
  __hip_atomic_store((lds_u32*)(lds + L_CTL) + c, v, __ATOMIC_RELAXED, __HIP_MEMORY_SCOPE_WORKGROUP);
  CB();
}
template <int SLEEP> __device__ __forceinline__ void await(unsigned char* lds, int c, unsigned n) {
  CB();
#pragma unroll 1
  for (unsigned spins = 0; spins < LSPIN_CAP; ++spins) {
    if (__builtin_amdgcn_readfirstlane(__hip_atomic_load((lds_u32*)(lds + L_CTL) + c, __ATOMIC_RELAXED, __HIP_MEMORY_SCOPE_WORKGROUP)) >= (int)n) break;
    if (SLEEP) __builtin_amdgcn_s_sleep(SLEEP);
  }
  CB();
}
template <int SLEEP> __device__ __forceinline__ void await_both(unsigned char* lds, int n) {
  CB();
#pragma unroll 1
  for (unsigned spins = 0; spins < LSPIN_CAP; ++spins) {
    const u64 v = __hip_atomic_load((__attribute__((address_space(3))) u64*)(lds + L_CTL + C_FULL * 4), __ATOMIC_RELAXED, __HIP_MEMORY_SCOPE_WORKGROUP);
    if (__builtin_amdgcn_readfirstlane(min((int)(unsigned)v, (int)(unsigned)(v >> 32))) >= n) break;
    if (SLEEP) __builtin_amdgcn_s_sleep(SLEEP);
  }
  CB();
}
__device__ __forceinline__ v2u trd(const unsigned char* p) {
  return __builtin_bit_cast(v2u, __builtin_amdgcn_ds_read_tr16_b64_v4i16((__attribute__((address_space(3))) s4*)p));
}
__device__ __forceinline__ v4i srd(const void* p, unsigned bytes) { const u64 a = (u64)p; return v4i{(int)(unsigned)a, (int)(unsigned)(a >> 32) & 0xffff, (int)bytes, 0x00020000}; }
#define DMW_ "s_waitcnt vmcnt(%8)\n\t"
#define DMA4_(PA, PB) asm volatile("s_mov_b32 m0, %1\n\tv_add_u32 %0, 2, %0\n\t" DMW_ PA "buffer_load_dwordx4 %2, %4, %5 offen nt lds\n\t" \
    DMW_ PB "buffer_load_dwordx4 %3, %4, %5 offen offset:1024 nt lds\n\t" \
    "v_add_u32 %0, 2, %0\n\t" DMW_ PA "buffer_load_dwordx4 %2, %4, %6 offen offset:2048 nt lds\n\t" \
    DMW_ PB "buffer_load_dwordx4 %3, %4, %6 offen offset:3072 nt lds" \
    : "+v"(cnt) : "s"(m0), "v"(v0), "v"(v1), "s"(r), "s"(oa), "s"(ob), "v"(addr), "i"(N) : "memory")
template <int N> __device__ __forceinline__ void dma4(int m0, int v0, int v1, v4i r, int oa, int ob, int addr, int& cnt) {
  if constexpr (N & 1) DMA4_("", "ds_write_b32 %7, %0\n\t");
  else DMA4_("ds_write_b32 %7, %0\n\t", "");
}
__device__ __forceinline__ void publish(int addr, int cnt) { asm volatile("ds_write_b32 %0, %1" :: "v"(addr), "v"(cnt) : "memory"); }

__global__ void __launch_bounds__(NTHR, 3) fused_decode(Params p) {
  __shared__ __attribute__((aligned(16))) unsigned char lds[L_END];
  const int tid = threadIdx.x, lane = tid & 63, wv = __builtin_amdgcn_readfirstlane(tid >> 6);
  const int j = blockIdx.x;
  const int G = lane >> 4, c16 = lane & 15;
  const int pair = j & 15, s = j >> 4;
  const int br = j >> 7, qh = ((j & 7) << 2) | ((j >> 3) & 3), kq = (j >> 5) & 3, ri = ((j >> 1) & 56) | (j & 7), ch = (j >> 3) & 1;
  const unsigned rot = (unsigned)j * 2654435761u >> 16;
  const unsigned KVB = 16u * SK * DH * 2, WB = DM * NQ * 2;

  const int lds0 = (int)(unsigned)(size_t)(__attribute__((address_space(3))) unsigned char*)lds;
  if (wv >= 2) {
  const int w = wv - 2;
  const u64 tc0 = __builtin_amdgcn_s_memrealtime();
  const bool hi8 = (lane & 8) != 0;
  const int b = pair >> 2, hkv = pair & 3;
  const f4 z = {0.f, 0.f, 0.f, 0.f};
  const v4u zu = {0, 0, 0, 0};
  if (wv == 2) {
    const int l = lane >> 5, f = lane & 31;
    const bool isO = f >= 24, is2 = (f >> 2) == 3, isK = f < 14, isB = s + (is2 ? 1 : 0) > 12;
    const u64 pk = isB ? (u64)p.kB : (u64)p.kA, pv = isB ? (u64)p.vB : (u64)p.vA, ptr = isO ? (u64)(br ? p.woB : p.woA) : isK ? pk : pv;
    const int oO = (256 * (ri >> 2) + 32 * (ri & 3)) * (DM * 2) + (512 * ch + 256 * l) * 2;
    const int rb = (4 * l + (rot >> 3)) * 2048;
    const int T = is2 ? 8 + ((f + pair) & 1) : (f + (isK ? 4 : 0) + pair * 5) & 7;
    const int ck = s > 12 ? 96 + T * 3 + (s - 13) : !is2 ? T * 13 + s : s == 12 ? 118 + T : 8 + T * 12 + s;
    const int o = isO ? oO + (((f + rot) & 3) << 12) : (pair * 128 + ck) << 14;
    *(v4i*)(lds + L_Q + lane * 32) = v4i{o + 2048 + (isO ? 0 : rb & 0x3800), o + (isO ? 128 : (rb + 2048) & 0x3800), o + 2048 + (isO ? 256 : (rb + 4096) & 0x3800),
                                         o + (isO ? 384 : (rb + 6144) & 0x3800)};
    *(v4i*)(lds + L_Q + lane * 32 + 16) = v4i{(int)(unsigned)(ptr - 2048), (int)(unsigned)((ptr - 2048) >> 32) & 0xffff, f >= 28 ? 0 : (isO ? (int)WB : (int)KVB) + 2048,
                                              lds0 + (f >= 28 ? L_QR : l * RING + (f % NSLOT) * 8192)};
    const int vK = G * 256 + ((c16 ^ (2 * G)) * 16), vO = (16 * (lane >> 5) + 128 * (G & 1) + (lane & 8)) * (DM * 2) + (lane & 7) * 16;
    *(v4i*)(lds + L_Q + 2048 + lane * 16) = v4i{vK, vK ^ 128, vO, (vO ^ 32) + DM * 2 - 1024};
    asm volatile("s_waitcnt lgkmcnt(0)" ::: "memory");
  }
  const v4u xa = __builtin_amdgcn_raw_buffer_load_b128(mk(br ? p.xB : p.xA, 4 * DM * 2), ((c16 >> 2) * DM + 8 * (2 * G + (c16 & 1))) * 2,
                                                       (256 * kq + 64 * (w >> 1)) * 2, 0);
  unsigned tch;
  {
    const size_t ko = (size_t)pair * (SK * DH * 2) + (SK - 1) * (DH * 2);
    const char* a = w == 1 ? (const char*)(s > 12 ? p.kB : p.kA) + ko : w == 2 ? (const char*)(s > 12 ? p.vB : p.vA) + ko
                  : w == 3 ? (const char*)(br ? p.woB : p.woA) + (size_t)(256 * (ri >> 2)) * (DM * 2) : w == 4 ? (const char*)p.ws + WS_PAD + 4096
                  : w == 5 ? (const char*)p.ws + WS_END : w == 6 ? (const char*)(s >= 12 ? p.kB : p.kA) + ko : w == 7 ? (const char*)(s >= 12 ? p.vB : p.vA) + ko : (const char*)p.ws + 2048;
    asm volatile("s_load_dword %0, %1, 0x0" : "=&s"(tch) : "s"(a) : "memory");
  }
  PIN();
  __builtin_amdgcn_s_barrier();
  PIN();
  asm volatile("s_waitcnt lgkmcnt(0)" :: "s"(tch) : "memory");
  const rsrc_t rws = mk(p.ws, WS_END);
  const u64 tag = launch_tag(p.ws);
  const unsigned tl = (unsigned)(tag >> (16 * (lane & 3)));
  unsigned char* mine = lds + (w >> 2) * RING + (w & 3) * 2048;
  const int kr0 = (lane & 7) * 256 + (((hi8 ? 4 : 0) + G) ^ (2 * (lane & 7))) * 16;
  const int cdone = C_DONE + w;
#define RD(sl, o) (*(const v4u*)(mine + (sl) * 8192 + (o)))
#define TAKE(f, N, dst, T, SL_, O0, O1, SLP) do { await<SLP>(lds, C_FULL + (w >> 2), (8 * ((f) + (N) - 1) + 2 * (w & 3) + 2)); \
    _Pragma("unroll") for (int u_ = 0; u_ < (N); ++u_) { const int t_ = (T) + u_; dst[2 * t_] = RD(SL_, O0); dst[2 * t_ + 1] = RD(SL_, O1); } \
    if ((f) + (N) <= 28 - NSLOT + 1) { asm volatile("s_waitcnt lgkmcnt(0)" ::: "memory"); setw(lds, cdone, (f) + (N)); } \
    _Pragma("unroll") for (int u_ = 0; u_ < 2 * (N); ++u_) asm volatile("" : "+v"(dst[2 * (T) + u_])); } while (0)
#define TAKEK(f, N, dst, T) TAKE(f, N, dst, T, ((f) + u_) % NSLOT, kr0 ^ ((t_ & 1) * 64), kr0 ^ ((t_ & 1) * 64) ^ 128, 2)
#define KEEP(x) asm volatile("" : "+v"(x))
#define UNTIL(t) do { _Pragma("unroll 1") while ((unsigned)(__builtin_amdgcn_s_memrealtime() - tc0) < (t)) __builtin_amdgcn_s_sleep(1); } while (0)
  v4u kB[4], kA[16], vt[8], wt[8];
  {
    TAKE(0, 4, vt, 0, (u_ - rot) & 3, lane * 16, lane * 16 + 1024, 1);
    tr64(vt, wt);
    f4 r = tile64(wt, xa, hi8);
    KEEP(r);
    *(f4*)(lds + L_QR + w * 1024 + lane * 16) = r;
    if ((unsigned)__builtin_amdgcn_readfirstlane(arrive(lds, C_QR, lane)) == 7u) {
      const int un = 2 * (lane & 7) + (hi8 ? 1 : 0);
      const unsigned tu = (unsigned)(tag >> (16 * (un & 3)));
      const unsigned char* q = lds + L_QR + lane * 16;
      v4u q0 = mark((*(const f4*)(q) + *(const f4*)(q + 2048)) + (*(const f4*)(q + 4096) + *(const f4*)(q + 6144)), tu);
      v4u q1 = mark((*(const f4*)(q + 1024) + *(const f4*)(q + 3072)) + (*(const f4*)(q + 5120) + *(const f4*)(q + 7168)), tu);
      KEEP(q0); KEEP(q1);
      UNTIL(TQS);
      stc(q0, rws, G * 512 + un * 16, WS_QP + ((br * 32 + qh) * 4 + kq) * 2048);
      stc(q1, rws, G * 512 + un * 16, WS_QP + ((br * 32 + qh) * 4 + kq) * 2048 + 256);
    }
  }
  TAKEK(4, 2, kA, 0); TAKEK(6, 2, kA, 2);
  TAKEK(8, 2, kA, 4);
  const bool mixed = s == 12;
  {
    const int qo = WS_QP + ((s + (w >> 2) > 12 ? 32 : 0) + hkv * 8 + 2 * (w & 3)) * 8192 + b * 512, qv = (lane >> 5) * 8192 + (lane & 31) * 16;
    const bool doq = w < 4 || mixed;
    v4u g[4] = {zu, zu, zu, zu};
    if (doq) {
#pragma unroll
      for (int u = 0; u < 4; ++u) g[u] = ldc(rws, qv, qo + u * 2048);
    }
    PIN();
    TAKEK(10, 2, kA, 6);
    if (doq) {
      settle<4, 8>(g, tl, lane, rws, qv, qo, 2048);
      const f4 q = (val(g[0]) + val(g[1])) + (val(g[2]) + val(g[3]));
      v2u qb = {pk2(q[0], q[1]), pk2(q[2], q[3])};
      KEEP(qb);
      *(v2u*)(lds + L_Q + (2 * w + (lane >> 5)) * QSTR + (lane & 31) * 8) = qb;
    }
    arrive(lds, C_Q, lane);
  }
  TAKEK(12, 2, kB, 0);
  await<1>(lds, C_Q, 8);
  constexpr float C2 = 1.4426950408889634f;
  float inv, lse;
  {
    const unsigned char* qp = lds + L_Q + (lane & 7) * QSTR + (hi8 ? 64 : 0) + G * 16;
    const bool t0lane = (G >> 1) == (c16 >> 3);
    const v4u qa0 = *(const v4u*)qp, qa1 = *(const v4u*)(qp + 128), qb0 = *(const v4u*)(qp + (mixed ? 8 * QSTR : 0)), qb1 = *(const v4u*)(qp + (mixed ? 8 * QSTR : 0) + 128);
    f4 d[10];
#pragma unroll
    for (int u = 0; u < 8; ++u) d[u] = mfma32(kA[2 * u], qa0, z);
    d[8] = mfma32(kB[0], qb0, z); d[9] = mfma32(kB[2], qb0, z);
#pragma unroll
    for (int u = 0; u < 8; ++u) d[u] = mfma32(kA[2 * u + 1], qa1, d[u]);
    d[8] = mfma32(kB[1], qb1, d[8]); d[9] = mfma32(kB[3], qb1, d[9]);
    PIN();
    float za[8], zb[2];
#pragma unroll
    for (int u = 0; u < 4; ++u) qk_fin(d[2 * u], d[2 * u + 1], t0lane, za[2 * u], za[2 * u + 1]);
    qk_fin(d[8], d[9], t0lane, zb[0], zb[1]);
    float mA = fmaxf(fmaxf(fmaxf(za[0], za[1]), fmaxf(za[2], za[3])), fmaxf(fmaxf(za[4], za[5]), fmaxf(za[6], za[7])));
    mA = max_x8_16_32(mA) * C2;
    const float mB = max_x8_16_32(fmaxf(zb[0], zb[1])) * C2;
    float lA = 0.f;
#pragma unroll
    for (int u = 0; u < 8; ++u) { za[u] = __builtin_amdgcn_exp2f(fmaf(za[u], C2, -mA)); lA += za[u]; }
    zb[0] = __builtin_amdgcn_exp2f(fmaf(zb[0], C2, -mB)); zb[1] = __builtin_amdgcn_exp2f(fmaf(zb[1], C2, -mB));
    lA = sum_x8_16_32(lA);
    const float lB = sum_x8_16_32(zb[0] + zb[1]);
    if (lane < 16) *(float2*)(lds + L_ML + (lane * 8 + w) * 8) = hi8 ? make_float2(mB, lB) : make_float2(mA, lA);
    arrive(lds, C_ML, lane);
    await<1>(lds, C_ML, 8);
    f4 mv[4];
#pragma unroll
    for (int u = 0; u < 4; ++u) mv[u] = *(const f4*)(lds + L_ML + c16 * 64 + u * 16);
    float Mw = fmaxf(fmaxf(fmaxf(mv[0][0], mv[0][2]), fmaxf(mv[1][0], mv[1][2])), fmaxf(fmaxf(mv[2][0], mv[2][2]), fmaxf(mv[3][0], mv[3][2])));
    float Mo = ror8(Mw);
    if (!mixed) Mo = Mw = fmaxf(Mw, Mo);
    float Lw = 0.f;
#pragma unroll
    for (int u = 0; u < 4; ++u) { Lw = fmaf(__builtin_amdgcn_exp2f(mv[u][0] - Mw), mv[u][1], Lw); Lw = fmaf(__builtin_amdgcn_exp2f(mv[u][2] - Mw), mv[u][3], Lw); }
    const float Lo = ror8(Lw);
    if (!mixed) Lw += Lo;
    inv = 1.0f / Lw;
    lse = Mw + __builtin_amdgcn_logf(Lw);
    const float fA = __builtin_amdgcn_exp2f(mA - (hi8 ? Mo : Mw)), fB = __builtin_amdgcn_exp2f(mB - (hi8 ? Mw : Mo));
    unsigned char* pw = lds + L_QR + (hi8 ? 1024 : 0) + (lane & 7) * 128 + ((w ^ (lane & 6)) * 16) + ((G ^ (2 * (w & 1))) * 4);
    {
#pragma unroll
      for (int u = 0; u < 4; ++u) *(unsigned*)(pw + u * 2048) = pk2(za[2 * u] * fA, za[2 * u + 1] * fA);
      *(unsigned*)(pw + 4 * 2048) = pk2(zb[0] * fB, zb[1] * fB);
    }
    arrive(lds, C_P, lane);
  }
  v4u pf[20];
  await<1>(lds, C_P, 8);
  {
    const unsigned char* pr = lds + L_QR + (lane & 7) * 128;
#pragma unroll
    for (int t = 0; t < 10; ++t) { pf[2 * t] = *(const v4u*)(pr + t * 1024 + ((G ^ (lane & 6)) * 16)); pf[2 * t + 1] = *(const v4u*)(pr + t * 1024 + (((4 + G) ^ (lane & 6)) * 16)); }
#pragma unroll
    for (int t = 0; t < 20; ++t) KEEP(pf[t]);
  }
  const int row0 = 2 * (c16 >> 2) + (G & 1), row1 = row0 ^ 1, chk = 2 * w + ((lane >> 1) & 1);
  const unsigned char* t0 = lds + G * 2048 + row0 * 256 + ((chk ^ (2 * row0)) * 16) + 8 * (lane & 1);
  const unsigned char* t1 = lds + G * 2048 + row1 * 256 + ((chk ^ (2 * row1)) * 16) + 8 * (lane & 1);
#define AFRAG(f, l) ({ const v2u x_ = trd(t0 + (l) * RING + ((f) % NSLOT) * 8192), y_ = trd(t1 + (l) * RING + ((f) % NSLOT) * 8192); v4u{x_[0], x_[1], y_[0], y_[1]}; })
#define PV(f, N, T, acc, SLP) do { await_both<SLP>(lds, (8 * ((f) + (N)))); v4u a_[2 * (N)]; \
    _Pragma("unroll") for (int u_ = 0; u_ < (N); ++u_) { a_[2 * u_] = AFRAG((f) + u_, 0); a_[2 * u_ + 1] = AFRAG((f) + u_, 1); } \
    if ((f) < 28 - NSLOT) { asm volatile("s_waitcnt lgkmcnt(0)" ::: "memory"); setw(lds, cdone, (f) + (N)); } \
    _Pragma("unroll") for (int u_ = 0; u_ < 2 * (N); ++u_) acc = mfma32(a_[u_], pf[2 * (T) + u_], acc); KEEP(acc); } while (0)
  f4 o2 = z;
  PV(14, 2, 8, o2, 1);
  f4 o1 = mixed ? z : o2;
  PV(16, 2, 0, o1, 1);
  if (s == 5 && w == 0) {
    const unsigned tb = (unsigned)__builtin_amdgcn_s_memrealtime(), te = tb + (tb - (unsigned)tc0) * ADP / 1024u;
    stc(v4u{te, (unsigned)tag, te, (unsigned)(tag >> 32)}, rws, lane == 0 ? pair * 128 : OOB, WS_PAD);
  }
  PV(18, 2, 2, o1, 1);
  const unsigned tg = (unsigned)(tag >> (16 * G)), xf = (G & 1) ? fu(lse) >> 16 : fu(lse);
  unsigned me = (tg & 0x0f0fu) | ((xf & 0x0f0fu) << 4), mo = ((tg >> 4) & 0x0f0fu) | (xf & 0xf0f0u);
  int sa = hi8 && !mixed ? OOB : (hi8 ? 4096 : 0) + (lane & 7) * 64 + G * 16;
  const bool e0 = lane < (br ? 32 : 64), e1 = lane < (br ? 0 : 40);
  int ovv[2] = {e0 ? (lane >> 3) * 4096 + (lane & 7) * 16 : OOB, e1 ? (8 + (lane >> 3)) * 4096 + (lane & 7) * 16 : OOB};
  unsigned tt[2] = {e0 ? tl : 0u, e1 ? tl : 0u}, xs = (lane & 1) ? 0x05040100u : 0x01000504u;
  float n0 = e0 ? 0.f : -INFINITY, n1 = e1 ? 0.f : -INFINITY;
  KEEP(xs); KEEP(n0); KEEP(n1);
  int oa = lds0 + L_O + ((w >> 1) * 8 + 4 * (w & 1) + ((lane & 7) >> 1)) * 16 + ((2 * (lane & 1) + (lane >> 5) - rot) & 3) * 4 + (G & 1) * 2;
  int ra = lds0 + L_O + ((c16 >> 2) * 8 + G) * 16, one = 1;
  int ca = lds0 + L_CTL + C_O * 4;
  int wa[8];
#pragma unroll
  for (int u = 0; u < 8; ++u) {
    const int q = c16 >> 2, sl = 6 + 2 * (u >> 2) + (q >> 1);
    wa[u] = (w >> 2) * RING + (w & 3) * 2048 + (sl == NSLOT ? 0 : sl) * 8192 + (q & 1) * 1024 + G * 128 + (q ^ (u & 3)) * 32 + (lane & 3) * 8;
    KEEP(wa[u]);
  }
  int pv = G * (16 * 8192) + c16 * 16;
  int fr = lds0 + L_Q + lane * 32, fa = fr + w * 4;
  int so = WS_OP + (pair * 17 + s + (s > 12)) * 4096 + w * 512, sp = WS_P2 + (br * 128 + 2 * ri + ch) * 8192 + w * 1024;
  int og = WS_OP + (((w >> 1) * 4 + (ri >> 4)) * 17 + br * 13) * 4096 + (2 * (ri & 3) + (w & 1)) * 512 + ((ri >> 2) & 3) * 128;
  int pg = WS_P2 + (br * 128 + 2 * w + ch) * 8192 + (ri & 31) * 256, xo = ((ri & 3) * 2048 + br * 1024 + 512 * ch + 64 * (ri >> 2)) * 2;
  KEEP(inv); KEEP(me); KEEP(mo); KEEP(sa); KEEP(ovv[0]); KEEP(ovv[1]); KEEP(tt[0]); KEEP(tt[1]); KEEP(oa); KEEP(ra); KEEP(one); KEEP(fr); KEEP(ca); KEEP(pv); KEEP(fa);
  asm volatile("" : "+s"(so), "+s"(sp), "+s"(og), "+s"(pg), "+s"(xo));
  unsigned cpf = 0;
  if (w == 1 && (s & 3) == 0) {
    u64 pc;
    asm volatile("s_getpc_b64 %0" : "=s"(pc));
    cpf = __builtin_amdgcn_raw_buffer_load_b32(mk((const void*)pc, CPF), lane * 64, 0, 0);
  }
  PIN();
  PV(20, 2, 4, o1, 1);
  v4u bc = zu;
  if (w == 0) bc = ldc(rws, lane < 16 ? lane * 128 : OOB, WS_PAD);
  PIN();
  PV(22, 1, 6, o1, 1);
  if (w == 0) {
    bool ok;
#pragma unroll 1
    for (;;) {
      ok = bc[1] == (unsigned)tag && bc[3] == (unsigned)(tag >> 32);
      if (!wave_bad(lane < 16 && !ok) || (unsigned)(__builtin_amdgcn_s_memrealtime() - tc0) > 2000u) break;
      __builtin_amdgcn_s_sleep(8);
      CB();
      const v4u nb = ldc(rws, lane < 16 && !ok ? lane * 128 : OOB, WS_PAD);
      if (!ok) bc = nb;
    }
    float dt = ok ? (float)(int)(bc[0] - (unsigned)tc0) : 0.f, dm = dt;
    dt += dpp<0xB1>(dt); dt += dpp<0x4E>(dt); dt += dpp<0x141>(dt); dt += ror8(dt);
    dm = mx(dm, dpp<0xB1>(dm)); dm = mx(dm, dpp<0x4E>(dm)); dm = mx(dm, dpp<0x141>(dm)); dm = mx(dm, ror8(dm));
    const int rel = min(max(__builtin_amdgcn_readfirstlane((int)(dt * ((100 - AMX) / 1600.f) + dm * (AMX / 100.f))), 1200), 2100);
    setw(lds, C_TG + 4, (unsigned)tc0 + rel);
    setw(lds, C_TG, 1);
  }
  await<1>(lds, C_TG, 1);
  const unsigned tga = __builtin_amdgcn_readfirstlane(__hip_atomic_load((lds_u32*)(lds + L_CTL) + C_TG + 4, __ATOMIC_RELAXED, __HIP_MEMORY_SCOPE_WORKGROUP));
  const unsigned tgd = max(min(tga - (unsigned)tc0, 2200u), 1000u);
  KEEP(cpf);
  UNTIL(tgd - TPD);
  PV(23, 1, 7, o1, 0);
  stc(mark8(f4{hi8 ? o2[0] : o1[0], hi8 ? o2[1] : o1[1], hi8 ? o2[2] : o1[2], hi8 ? o2[3] : o1[3]} * inv, me, mo), rws, sa, so);
  await<0>(lds, C_ISS, 27); await<0>(lds, C_ISS + 1, 27);
  unsigned pre1 = __builtin_amdgcn_raw_buffer_load_b32(rws, lane < 8 ? lane * 128 : OOB, sp, AUX_SC1);
  PIN();
  {
    v4u g[2];
    UNTIL(tgd);
    g[0] = ldc(rws, ovv[0], og); g[1] = ldc(rws, ovv[1], og);
    PIN();
    await<0>(lds, C_FULL + (w >> 2), 8 * 27 + 2 * (w & 3) + 2);
#pragma unroll
    for (int u = 0; u < 8; ++u) {
      const v2u x = trd(lds + wa[u & 3] + (u >> 2) * 512), yv = trd(lds + wa[4 + (u & 3)] + (u >> 2) * 512);
      wt[u] = v4u{x[0], x[1], yv[0], yv[1]};
    }
#pragma unroll
    for (int u = 0; u < 8; ++u) KEEP(wt[u]);
    PIN();
    settle2<1>(g, tt, lane, rws, ovv, og);
    KEEP(pre1);
    const float x0 = lse8(g[0], xs) + n0, x1 = lse8(g[1], xs) + n1;
    const float M = max_x8_16_32(mx(x0, x1));
    const float w0 = __builtin_amdgcn_exp2f(x0 - M), w1 = __builtin_amdgcn_exp2f(x1 - M);
    const float y = fold4(val8(g[0]) * w0 + val8(g[1]) * w1) * __builtin_amdgcn_rcpf(sum_x8_16_32(w0 + w1));
    asm volatile("ds_write_b16 %0, %1\n\ts_mov_b64 exec, 1\n\tds_add_u32 %2, %3\n\ts_mov_b64 exec, -1" :: "v"(oa), "v"(pk2(y, y)), "v"(ca), "v"(one) : "memory");
    v4u a0, a1;
#pragma unroll 1
    for (unsigned spins = 0; spins < LSPIN_CAP; ++spins) {
      int c;
      asm volatile("ds_read_b32 %0, %3\n\tds_read_b128 %1, %4\n\tds_read_b128 %2, %4 offset:64\n\ts_waitcnt lgkmcnt(0)" : "=&v"(c), "=&v"(a0), "=&v"(a1) : "v"(ca), "v"(ra) : "memory");
      if (__builtin_amdgcn_readfirstlane(c) >= 8) break;
    }
    f4 d[4];
#pragma unroll
    for (int n = 0; n < 4; ++n) d[n] = mfma32(a0, wt[n], z);
    PIN();
#pragma unroll
    for (int n = 0; n < 4; ++n) d[n] = mfma32(a1, wt[4 + n], d[n]);
    PIN();
    stc(mark(f4{d[0][0], d[1][0], d[2][0], d[3][0]}, tl), rws, lane * 16, sp);
  }
  if (ri < 32) {
    __builtin_amdgcn_s_sleep(D2);
    v4u g[2] = {ldc(rws, pv, pg), ldc(rws, pv, pg + 64 * 8192)};
    settle<2, 0>(g, tl, lane, rws, pv, pg, 64 * 8192);
    const f4 v = val(g[0]) + val(g[1]);
    int old;
    f4 u0, u1;
    asm volatile("ds_write_b32 %3, %4\n\ts_mov_b64 exec, 1\n\tds_add_rtn_u32 %0, %5, %6 offset:%8\n\ts_mov_b64 exec, -1\n\t"
                 "ds_read_b128 %1, %7\n\tds_read_b128 %2, %7 offset:16\n\ts_waitcnt lgkmcnt(0)"
                 : "=&v"(old), "=&v"(u0), "=&v"(u1) : "v"(fa), "v"(red16(red32(v[0], v[2]), red32(v[1], v[3]))), "v"(ca), "v"(one), "v"(fr), "i"((C_F - C_O) * 4) : "memory");
    if (__builtin_amdgcn_readfirstlane(old) == 7) {
      const float r = ((u0[0] + u0[1]) + (u0[2] + u0[3])) + ((u1[0] + u1[1]) + (u1[2] + u1[3]));
      __builtin_amdgcn_raw_buffer_store_b16((short)pk2(r, r), mk(p.out, 4 * 2048 * 2), lane * 2, xo, AUX_SC1);
      if (j == 0) __builtin_amdgcn_raw_buffer_store_b64(v2u{(unsigned)tag, (unsigned)(tag >> 32)}, rws, lane == 0 ? 0 : OOB, WS_EPOCH, AUX_SC1);
    }
  }
  return;
  }

  {
    __builtin_amdgcn_s_setprio(3);
    const int l = wv;
    const int full_addr = lds0 + L_CTL + (C_FULL + l) * 4, done_addr = lds0 + L_CTL + C_DONE * 4;
    const int ring0 = lds0 + l * RING;
    int v0 = (8 * (lane >> 3)) * (NQ * 2) + (lane & 7) * 16, v1 = v0 + NQ * 2 - 1024;
    int qb = (256 * kq + 128 * l + 1) * (NQ * 2) + qh * 256;
    int qa = qb + ((rot & 3) << 14), qc = qb + (((rot + 1) & 3) << 14), cnt = -2 - (WHI & ~1), fa0 = full_addr;
    asm volatile("" : "+v"(v0), "+v"(v1), "+v"(cnt), "+v"(fa0));
    asm volatile("" : "+s"(qa), "+s"(qc));
    v4i tb = srd((const char*)(br ? p.wqB : p.wqA) - NQ * 2, WB + NQ * 2);
    dma4<WHI>(ring0, v0, v1, tb, qa, qa + 128 - 2048, fa0, cnt);
    dma4<WHI>(ring0 + 4096, v0, v1, tb, qa + 64 * NQ * 2, qa + 64 * NQ * 2 + 128 - 2048, fa0, cnt);
    dma4<WHI>(ring0 + 8192, v0, v1, tb, qc, qc + 128 - 2048, fa0, cnt);
    dma4<WHI>(ring0 + 12288, v0, v1, tb, qc + 64 * NQ * 2, qc + 64 * NQ * 2 + 128 - 2048, fa0, cnt);
    int ln = lane;
    asm volatile("" : "+v"(ln));
    publish(lds0 + L_CTL + ((ln & 30) == C_FULL ? 0 : (ln & 31)) * 4, 0);
    v4i vp = {v0, v1, 0, 0};
    const u64 tl0 = __builtin_amdgcn_s_memrealtime();
    int wide = WHI - WND, freed = 4;
    const int q0 = qb + (((ln + rot) & 3) << 14);
    v4i ta = {q0, q0 + 128 - 2048, q0 + 64 * NQ * 2, q0 + 64 * NQ * 2 + 128 - 2048};
    tb[3] = ring0 + ln * 8192;
#pragma unroll 1
    for (int f = 2; f < 31; ++f) {
      v4i r = {__builtin_amdgcn_readlane(tb[0], f), __builtin_amdgcn_readlane(tb[1], f), __builtin_amdgcn_readlane(tb[2], f), 0x00020000};
      int o0 = __builtin_amdgcn_readlane(ta[0], f), o1 = __builtin_amdgcn_readlane(ta[1], f);
      int dst = __builtin_amdgcn_readlane(tb[3], f), o2 = __builtin_amdgcn_readlane(ta[2], f), o3 = __builtin_amdgcn_readlane(ta[3], f);
      asm volatile("s_nop 2" : "+s"(r), "+s"(o0), "+s"(o1), "+s"(o2), "+s"(o3), "+s"(dst));
      if (wide) {
        dma4<WHI>(dst, v0, v1, r, o0, o1, full_addr, cnt);
        dma4<WHI>(dst + 4096, v0, v1, r, o2, o3, full_addr, cnt);
      } else {
        dma4<WND>(dst, v0, v1, r, o0, o1, full_addr, cnt);
        dma4<WND>(dst + 4096, v0, v1, r, o2, o3, full_addr, cnt);
      }
      if (f == 2 || f == 23) {
        cnt += wide;
        wide = (unsigned)(__builtin_amdgcn_s_memrealtime() - tl0) >= TL ? WHI - WND : 0;
        cnt -= wide;
      }
      publish(full_addr + (C_ISS - C_FULL) * 4, f);
      asm volatile("s_cmp_lg_u32 %3, 3\n\ts_cbranch_scc1 1f\n\ts_waitcnt lgkmcnt(0)\n\ts_barrier\n\t"
                   "ds_read_b128 %0, %4\n\tds_read_b128 %1, %4 offset:16\n\tds_read_b128 %2, %5\n\ts_waitcnt lgkmcnt(0)\n1:"
                   : "+v"(ta), "+v"(tb), "+v"(vp) : "s"(f), "v"(lds0 + L_Q + l * 1024 + (ln & 31) * 32), "v"(lds0 + L_Q + 2048 + ln * 16) : "scc", "memory");
      v0 = f < 23 ? vp[0] : vp[2] ^ (~f & 1) << 6;
      v1 = f < 23 ? vp[1] : vp[3] ^ (~f & 1) << 6;
      if (f + 2 >= freed) {
#pragma unroll 1
        for (unsigned spins = 0; spins < LSPIN_CAP; ++spins) {
          v4i d, e;
          asm volatile("ds_read_b128 %0, %2\n\tds_read_b128 %1, %2 offset:16\n\ts_waitcnt lgkmcnt(0)" : "=&v"(d), "=&v"(e) : "v"(done_addr) : "memory");
          freed = NSLOT + __builtin_amdgcn_readfirstlane(min(min(min(d[0], d[1]), min(d[2], d[3])), min(min(e[0], e[1]), min(e[2], e[3]))));
          if (freed >= 28) freed = 64;
          if (__builtin_expect(f + 1 < freed, 1)) break;
          asm volatile("s_waitcnt vmcnt(0)" ::: "memory");
          publish(full_addr, 8 * f + 8);
          __builtin_amdgcn_s_sleep(1);
        }
      }
    }
    asm volatile("s_waitcnt vmcnt(0)" ::: "memory");
  }

}

extern "C" void kernel_launch(void* const* d_in, const int* in_sizes, int n_in, void* d_out, int out_size, void* d_ws, size_t ws_size,
                              hipStream_t stream) {
  Params p;
  p.xA = d_in[0]; p.kA = d_in[1]; p.vA = d_in[2]; p.wqA = d_in[3]; p.woA = d_in[4];
  p.xB = d_in[5]; p.kB = d_in[6]; p.vB = d_in[7]; p.wqB = d_in[8]; p.woB = d_in[9];
  p.out = d_out; p.ws = d_ws;
  fused_decode<<<NWG, NTHR, 0, stream>>>(p);
}
```
